```python
import torch

def _softmax(x, axis=-1):
    return torch.softmax(x, dim=axis)
def _gelu(x):
    return torch.nn.functional.gelu(x, approximate="tanh")
def _silu(x):
    return torch.nn.functional.silu(x)
def _sigmoid(x):
    return torch.sigmoid(x)
def _layernorm(x, axis=-1, eps=1e-5):
    m = x.mean(dim=axis, keepdim=True)
    v = x.var(dim=axis, keepdim=True, unbiased=False)
    return (x - m) / torch.sqrt(v + eps)
def _scatter_add(tgt, idx, src, axis=0):
    return tgt.clone().index_add_(axis, idx, src)
def _segment_sum(src, seg, n):
    out = torch.zeros((n,) + src.shape[1:], dtype=src.dtype)
    return out.index_add_(0, seg, src)
def _pad(x, pw):
    flat = tuple(v for pair in reversed(pw) for v in pair)
    return torch.nn.functional.pad(x, flat)

kernel_name = 'standalone__gqa_block_cached__near_ridge'

def setup_inputs():

    _w = (lambda a: (lambda t: t.to(torch.bfloat16) if t.is_floating_point() else t)(torch.as_tensor(a)))
    B, Sq, D = 16, 64, 640
    Hq, Hkv, Sk, Dh = 32, 32, 512, 192
    assert Hq % Hkv == 0, (Hq, Hkv)
    return {
        "x": _w((lambda *s: torch.randn(*s, dtype=torch.bfloat16) * 0.8)(B, Sq, D)),
        "k_cache": _w((lambda *s: torch.randn(*s, dtype=torch.bfloat16) * 0.8)(B, Hkv, Sk, Dh)),
        "v_cache": _w((lambda *s: torch.randn(*s, dtype=torch.bfloat16) * 0.05)(B, Hkv, Sk, Dh)),
        "wq": _w((lambda *s: torch.randn(*s, dtype=torch.bfloat16) * 0.8)(D, Hq * Dh) * (D ** -0.5)),
        "wo": _w((lambda *s: torch.randn(*s, dtype=torch.bfloat16) * 0.05)(Hq * Dh, D) * ((Hq * Dh) ** -0.5)),
    }

def reference(x, k_cache, v_cache, wq, wo):
    B, Sq, D = x.shape
    Hq, Dh = 32, 192
    Hkv = k_cache.shape[1]
    rep = Hq // Hkv
    q = (lambda x, *d: x.permute(*d))((x @ wq).reshape(B, Sq, Hq, Dh), 0, 2, 1, 3)
    k = (lambda x, n, axis: torch.repeat_interleave(x, n, dim=axis))(k_cache, rep, axis=1)
    v = (lambda x, n, axis: torch.repeat_interleave(x, n, dim=axis))(v_cache, rep, axis=1)
    scores = torch.einsum("bhqd,bhkd->bhqk", q, k) * (Dh ** -0.5)
    p = _softmax(scores, axis=-1)
    o = torch.einsum("bhqk,bhkd->bhqd", p, v)
    return (lambda x, *d: x.permute(*d))(o, 0, 2, 1, 3).reshape(B, Sq, Hq * Dh) @ wo
```

```cpp
#include <hip/hip_runtime.h>
#include <hip/hip_bf16.h>
using bf16 = __hip_bfloat16;
constexpr int B=16,SQ=64,D=640,H=32,SK=512,DH=192;
__device__ __forceinline__ float b2f(bf16 v){return __bfloat162float(v);}
__global__ void qproj(const bf16*x,const bf16*wq,bf16*q){
  int row=blockIdx.x;
  __shared__ float xs[D];
  for(int i=threadIdx.x;i<D;i+=blockDim.x) xs[i]=b2f(x[row*D+i]);
  __syncthreads();
  for(int n=threadIdx.x;n<H*DH;n+=blockDim.x){
    float a=0; for(int c=0;c<D;++c) a+=xs[c]*b2f(wq[(size_t)c*H*DH+n]);
    q[(size_t)row*H*DH+n]=__float2bfloat16(a);
  }
}
__global__ void attn(const bf16*q,const bf16*k,const bf16*v,bf16*o){
  int s=blockIdx.x, h=blockIdx.y, b=blockIdx.z;
  __shared__ float qs[DH]; __shared__ float p[SK]; __shared__ float red[256];
  int t=threadIdx.x;
  if(t<DH) qs[t]=b2f(q[((size_t)(b*SQ+s))*H*DH+h*DH+t]);
  __syncthreads();
  const bf16*kb=k+((size_t)(b*H+h))*SK*DH; const bf16*vb=v+((size_t)(b*H+h))*SK*DH;
  float m=-1e30f;
  for(int j=t;j<SK;j+=256){float a=0;for(int d=0;d<DH;++d)a+=qs[d]*b2f(kb[j*DH+d]); a*=0.07216878364870322f; p[j]=a; m=fmaxf(m,a);}
  red[t]=m; __syncthreads();
  for(int o2=128;o2>0;o2>>=1){ if(t<o2) red[t]=fmaxf(red[t],red[t+o2]); __syncthreads();}
  m=red[0]; __syncthreads();
  float sum=0; for(int j=t;j<SK;j+=256){float e=__expf(p[j]-m); p[j]=e; sum+=e;}
  red[t]=sum; __syncthreads();
  for(int o2=128;o2>0;o2>>=1){ if(t<o2) red[t]+=red[t+o2]; __syncthreads();}
  sum=red[0];
  if(t<DH){float a=0; for(int j=0;j<SK;++j)a+=p[j]*b2f(vb[j*DH+t]); o[((size_t)(b*SQ+s))*H*DH+h*DH+t]=__float2bfloat16(a/sum);}
}
__global__ void oproj(const bf16*o,const bf16*wo,bf16*out){
  int row=blockIdx.x;
  __shared__ float os[H*DH];
  for(int i=threadIdx.x;i<H*DH;i+=blockDim.x) os[i]=b2f(o[(size_t)row*H*DH+i]);
  __syncthreads();
  for(int n=threadIdx.x;n<D;n+=blockDim.x){
    float a=0; for(int c=0;c<H*DH;++c) a+=os[c]*b2f(wo[(size_t)c*D+n]);
    out[row*D+n]=__float2bfloat16(a);
  }
}
extern "C" void kernel_launch(void* const* d_in, const int* in_sizes, int n_in, void* d_out, int out_size, void* d_ws, size_t ws_size, hipStream_t stream){
  const bf16*x=(const bf16*)d_in[0],*k=(const bf16*)d_in[1],*v=(const bf16*)d_in[2],*wq=(const bf16*)d_in[3],*wo=(const bf16*)d_in[4];
  bf16*q=(bf16*)d_ws; bf16*o=q+(size_t)B*SQ*H*DH;
  qproj<<<B*SQ,256,0,stream>>>(x,wq,q);
  attn<<<dim3(SQ,H,B),256,0,stream>>>(q,k,v,o);
  oproj<<<B*SQ,320,0,stream>>>(o,wo,(bf16*)d_out);
}
```

```cpp
#include <hip/hip_runtime.h>
#include <hip/hip_bf16.h>
#include <stdint.h>

typedef short s4 __attribute__((ext_vector_type(4)));
typedef short s8 __attribute__((ext_vector_type(8)));
typedef __bf16 bf8 __attribute__((ext_vector_type(8)));
typedef __bf16 bf2 __attribute__((ext_vector_type(2)));
typedef float f2 __attribute__((ext_vector_type(2)));
typedef float f4 __attribute__((ext_vector_type(4)));
typedef float f16v __attribute__((ext_vector_type(16)));
typedef unsigned u2 __attribute__((ext_vector_type(2)));
typedef unsigned u4 __attribute__((ext_vector_type(4)));
typedef unsigned long long u64;
#define LAS __attribute__((address_space(3)))
#define DI __device__ __forceinline__

template <int N> struct IC { static constexpr int v = N; };
template <int A, int B, class F> DI void sfor(F&& f) { if constexpr (A < B) { f(IC<A>{}); sfor<A + 1, B>(f); } }

constexpr unsigned US = 16384, KVS = 32768, XB = 65536, YB = 98304, HB = 147456, LDS_BYTES = 163840;
constexpr unsigned ITEM = 196608;
constexpr float SC = 0.07216878364870322f * 1.4426950408889634f;

#define PROJ_PRIO 0
#define ATT_PRIO 0
#define PART_AUX 0
#define STAMP(i) do {} while (0)
#define STAMP_DECL
#define STAMP_FLUSH() do {} while (0)

#define NOPS
template <class T> DI T rfl(T v) { return v; }
DI u4 mk_srd(const void* p, unsigned bytes) {
  const u64 a = (u64)p;
  u4 r = {(unsigned)__builtin_amdgcn_readfirstlane((unsigned)a), (unsigned)__builtin_amdgcn_readfirstlane((unsigned)(a >> 32) & 0xffffu), bytes, 0x00020000u};
  return r;
}
template <bool NT> DI void dma4(unsigned mb, unsigned v0, unsigned v1, u4 srd, unsigned so, unsigned st) {
  unsigned t;
#define DMA4_BODY(POL) \
  asm volatile(NOPS "s_mov_b32 m0, %1\n\ts_add_u32 %0, %5, %6\n\tbuffer_load_dwordx4 %2, %4, %5 offen " POL "lds\n\t" \
               "s_add_u32 m0, %1, 0x400\n\ts_nop 0\n\tbuffer_load_dwordx4 %3, %4, %0 offen " POL "lds\n\t" \
               "s_add_u32 m0, %1, 0x800\n\ts_add_u32 %0, %0, %6\n\tbuffer_load_dwordx4 %2, %4, %0 offen " POL "lds\n\t" \
               "s_add_u32 m0, %1, 0xc00\n\ts_add_u32 %0, %0, %6\n\tbuffer_load_dwordx4 %3, %4, %0 offen " POL "lds" \
               : "=&s"(t) : "s"(rfl(mb)), "v"(v0), "v"(v1), "s"(rfl(srd)), "s"(rfl(so)), "s"(rfl(st)) : "memory", "scc")
  if constexpr (NT) DMA4_BODY("nt "); else DMA4_BODY("");
#undef DMA4_BODY
}
template <int J> DI void dma1nt(unsigned mb, unsigned voff, u4 srd, unsigned so) {
  unsigned t;
  asm volatile(NOPS "s_add_u32 m0, %1, %2\n\ts_add_u32 %0, %3, %4\n\tbuffer_load_dwordx4 %5, %6, %0 offen nt lds"
               : "=&s"(t) : "s"(rfl(mb)), "i"(J * 1024), "s"(rfl(so)), "i"(J * 3072), "v"(voff), "s"(rfl(srd)) : "memory", "scc");
}
DI void dma1(unsigned m0v, unsigned voff, u4 srd, unsigned so) {
  asm volatile("s_mov_b32 m0, %0\n\ts_nop 0\n\tbuffer_load_dwordx4 %1, %2, %3 offen lds" :: "s"(m0v), "v"(voff), "s"(srd), "s"(so) : "memory");
}
DI void touch4(unsigned m0v, unsigned voff, u4 srd, unsigned so) {
  asm volatile("s_mov_b32 m0, %0\n\ts_nop 0\n\tbuffer_load_dword %1, %2, %3 offen lds" :: "s"(m0v), "v"(voff), "s"(srd), "s"(so) : "memory");
}
#define SB() __builtin_amdgcn_sched_barrier(0)
template <int N> DI void bar_v() { SB(); asm volatile("s_waitcnt vmcnt(%0) lgkmcnt(0)\n\ts_barrier" ::"i"(N) : "memory"); SB(); }
DI void bar_l() { SB(); asm volatile("s_waitcnt lgkmcnt(0)\n\ts_barrier" ::: "memory"); SB(); }

DI bf8 ld128(LAS char* sm, unsigned a) { return __builtin_bit_cast(bf8, *(LAS s8*)(sm + a)); }
DI bf8 ldtr(LAS char* sm, unsigned a0, unsigned a1) {
  s4 x = __builtin_amdgcn_ds_read_tr16_b64_v4i16((LAS s4*)(sm + a0)), y = __builtin_amdgcn_ds_read_tr16_b64_v4i16((LAS s4*)(sm + a1));
  return __builtin_bit_cast(bf8, __builtin_shufflevector(x, y, 0, 1, 2, 3, 4, 5, 6, 7));
}
DI unsigned opq(unsigned v) { asm volatile("" : "+v"(v)); return v; }
DI unsigned sel(bool c, unsigned a, unsigned b) { return c ? a : b; }
DI int opqs(int v) { v = __builtin_amdgcn_readfirstlane(v); asm volatile("" : "+s"(v)); return v; }
DI int lane_id() { unsigned z = 0; asm volatile("" : "+v"(z)); return __builtin_amdgcn_mbcnt_hi(~0u, __builtin_amdgcn_mbcnt_lo(~0u, z)); }
DI unsigned pk(float a, float b) { f2 x = {a, b}; return __builtin_bit_cast(unsigned, __builtin_convertvector(x, bf2)); }
DI f4 mfma16(bf8 a, bf8 b, f4 c) { return __builtin_amdgcn_mfma_f32_16x16x32_bf16(a, b, c, 0, 0, 0); }
DI f16v mfma32(bf8 a, bf8 b, f16v c) { return __builtin_amdgcn_mfma_f32_32x32x16_bf16(a, b, c, 0, 0, 0); }

constexpr u64 w_table() { u64 t = 0; for (int c = 0; c < 24; ++c) { const int n = c < 12 ? 2 + (c & 1) : (c == 13 || c == 16 || c == 19 || c == 21 || c == 23) ? 2 : (c == 14 || c == 17 || c == 20 || c == 22) ? 3 : 0; t |= (u64)n << (2 * c); } return t; }

__global__ __launch_bounds__(512, 2) void fused_kernel(const char* __restrict__ x, const char* __restrict__ K, const char* __restrict__ V,
                                                       const char* __restrict__ wq, const char* __restrict__ wo, char* __restrict__ part
) {
  __shared__ __attribute__((aligned(16))) char smem[LDS_BYTES];
  LAS char* sm = (LAS char*)smem;
  const int wave = __builtin_amdgcn_readfirstlane(threadIdx.x >> 6), w = wave & 3;
  const int xcd = blockIdx.x & 7, idx = blockIdx.x >> 3, b = idx >> 1, hp = idx & 1, h0 = xcd * 4 + hp * 2, mem = xcd * 2 + hp;
  const u4 sK = mk_srd(K + (size_t)(b * 32 + h0) * ITEM, 2 * ITEM), sV = mk_srd(V + (size_t)(b * 32 + h0) * ITEM, 2 * ITEM);
  const unsigned w4k = w * 4096u, OOB = 0x7ffffff0u;
  auto v_k = [&](int l) { return (unsigned)((32 * w + (l >> 3)) * 384 + (((l & 7) ^ (l >> 4)) << 4)); };

  if (wave < 4) {
    __builtin_amdgcn_s_setprio(ATT_PRIO);
    const int lane = lane_id();
    STAMP_DECL; STAMP(0);
    const int r16 = lane & 15, g = lane >> 4, q4 = (lane & 15) >> 2, p = lane & 3;
    const u4 sQ = mk_srd(wq + h0 * 384, 640 * 12288 - h0 * 384), sO = mk_srd(wo + (size_t)h0 * 192 * 1280, 384 * 1280);
    auto v_tq = [&]() { const unsigned l = lane_id(), tl = b * 120 + w * 30 + l; return l < 30 ? (tl / 3) * 12288 + (tl % 3) * 128 : OOB; };
    auto v_to = [&]() { const unsigned l = lane_id(), tl = b * 120 + w * 30 + l; return l < 30 ? tl * 128 : OOB; };
    unsigned w_so = 0, w_g = 0, w_v = (32 * w + (lane >> 3)) * 12288 + (((lane & 7) ^ (((lane >> 4) & 1) << 2)) << 4), w_st = 8 * 12288, w_dT = 128 * 12288, w_dG = 128u - 5u * 128 * 12288;
    u4 w_srd = sQ;
    auto w_tile = [&](unsigned lb) { dma4<false>(lb + w4k, w_v, w_v, w_srd, w_so, w_st); w_so += w_dT; };
    auto w_pair = [&]() { w_tile(XB); w_tile(XB + US); };
    auto w_triple = [&]() { w_tile(YB); w_tile(YB + US); w_tile(YB + 2 * US); w_so += w_dG;
      if (++w_g == 6) { const int l = lane_id(); w_so = 0; w_v = (16 * w + (l >> 4)) * 1280 + (((l & 15) ^ ((l >> 4) << 2)) << 4); w_st = 4 * 1280; w_dT = 256; w_dG = 64 * 1280 - 5 * 256; w_srd = sO; } };
    auto w_issue = [&](int c) { const unsigned n = (unsigned)(w_table() >> (2 * c + 2)) & 3; if (n == 2) w_pair(); else if (n == 3) w_triple(); };
    { const u4 sX = mk_srd(x + b * 81920, 81920);
      const unsigned vX = (16 * w + (lane >> 4)) * 1280 + (((lane & 15) ^ (lane >> 4)) << 4);
      const unsigned t0 = (hp ? HB : 0) + w4k;
      touch4(t0, v_tq(), sQ, 0);
      { const unsigned vt = lane < 2 ? lane * ITEM : OOB;
        if (w == 0) touch4(t0, vt, sK, 0); else if (w == 1) touch4(t0, vt, sV, 0); else if (w == 2) touch4(t0, vt, sO, 0); else touch4(t0, vt, mk_srd(part + (size_t)(b * 16 + mem) * 81920, 4096), 0); }
      for (int t = 0; t < 5; ++t) { const unsigned tt = hp ? 4 - t : t, lb = tt == 4 ? HB : tt * US;
        sfor<0, 4>([&](auto j_) { constexpr int j = decltype(j_)::v; dma1(lb + w4k + j * 1024, vX ^ (j << 6), sX, tt * 256 + j * 4 * 1280); }); }
      w_pair(); w_triple();
      STAMP(104);
      bar_v<20>();
      STAMP(105);
      bar_l();
      { const unsigned vK = v_k(lane); for (int u = 0; u < 2; ++u) dma4<true>(u * US + w4k, vK, vK ^ 64, sK, u * 49152, 3072); }
      STAMP(106);
      bar_v<8>();
      STAMP(107);
      bar_l();
      bar_l();
      w_pair();
      bar_v<8>();
      STAMP(1); }
    const unsigned ka0 = r16 * 128 + ((g ^ ((r16 >> 1) & 7)) << 4);
    const unsigned va0 = (4 * g + q4) * 128 + p * 8 + (((2 * g + (q4 >> 1)) & 3) << 5);
    const unsigned wa0 = (16 * w + r16) * 128 + (((g >> 1) ^ ((r16 >> 1) & 7)) << 4) + (g & 1) * 8 + HB;
    bf8 qc[2], qn[3][2], pf[16]; f4 S[32], O[4], Ls; u2 hA[4]; float m0, mc, alpha, linv;
    const u4 ones_u = {0x3f803f80u, 0x3f803f80u, 0x3f803f80u, 0x3f803f80u};
    auto read_q = [&](bf8* q, int hs) { const unsigned a = opq(ka0 + HB + w * 2048 + hs * 8192); q[0] = ld128(sm, a); q[1] = ld128(sm, a ^ 64); };
    auto k_tile = [&](auto kb_, auto first_) { constexpr int kb = decltype(kb_)::v, PD = 8; constexpr bool first = decltype(first_)::v;
      const unsigned a0 = opq(ka0) + kb * KVS, a1 = a0 ^ 64;
      bf8 a[32];
      auto rd = [&](auto i_) { constexpr int i = decltype(i_)::v; a[i] = ld128(sm, ((i & 1) ? a1 : a0) + (i >> 1) * 2048); };
      sfor<0, PD>(rd); SB();
      sfor<0, 16>([&](auto j_) { constexpr int j = decltype(j_)::v, blk = 16 * kb + j;
        f4 c; if constexpr (first) c = f4{0, 0, 0, 0}; else c = S[blk];
        c = mfma16(a[2 * j + 1], qc[1], c); S[blk] = mfma16(a[2 * j], qc[0], c);
        if constexpr (2 * j + PD < 32) { rd(IC<2 * j + PD>{}); rd(IC<2 * j + PD + 1>{}); }
        SB(); }); };
    auto max_half = [&](auto h_) { constexpr int h = decltype(h_)::v;
      float m = fmaxf(S[16 * h][0], S[16 * h][1]), m2 = fmaxf(S[16 * h][2], S[16 * h][3]);
      sfor<1, 16>([&](auto b_) { constexpr int i = 16 * h + decltype(b_)::v; m = fmaxf(fmaxf(m, S[i][0]), S[i][1]); m2 = fmaxf(fmaxf(m2, S[i][2]), S[i][3]); });
      m = fmaxf(m, m2);
      { auto r = __builtin_amdgcn_permlane16_swap(__float_as_uint(m), __float_as_uint(m), false, false); m = fmaxf(__uint_as_float(r[0]), __uint_as_float(r[1])); }
      { auto r = __builtin_amdgcn_permlane32_swap(__float_as_uint(m), __float_as_uint(m), false, false); m = fmaxf(__uint_as_float(r[0]), __uint_as_float(r[1])); }
      if constexpr (h == 0) m0 = m; else { m = fmaxf(m, m0); alpha = __builtin_amdgcn_exp2f((m0 - m) * SC); }
      mc = -m * SC; };
    auto exp_kk = [&](auto kk_) { constexpr int kk = decltype(kk_)::v; float e[8];
      sfor<0, 8>([&](auto j_) { constexpr int j = decltype(j_)::v; e[j] = __builtin_amdgcn_exp2f(fmaf(S[2 * kk + (j >> 2)][j & 3], SC, mc)); });
      u4 u = {pk(e[0], e[1]), pk(e[2], e[3]), pk(e[4], e[5]), pk(e[6], e[7])}; pf[kk] = __builtin_bit_cast(bf8, u); };
    auto v_tile = [&](auto kb_, auto ls_) { constexpr int kb = decltype(kb_)::v, PD = 6; constexpr bool ls = decltype(ls_)::v;
      const unsigned a0 = opq(va0) + kb * KVS;
      bf8 a[32];
      auto rd = [&](auto i_) { constexpr int i = decltype(i_)::v; const unsigned ad = (a0 ^ ((i & 3) << 5)) + (i >> 2) * 4096; a[i] = ldtr(sm, ad, ad + 2048); };
      sfor<0, PD>(rd);
      if constexpr (kb == 1) { sfor<0, 4>([&](auto d_) { constexpr int db = decltype(d_)::v; O[db] *= alpha; }); if constexpr (ls) Ls *= alpha; }
      SB();
      sfor<0, 16>([&](auto j_) { constexpr int j = decltype(j_)::v, kk = j >> 1, d0 = (j & 1) * 2;
        f4 c0, c1; if constexpr (kb == 0 && kk == 0) { c0 = f4{0, 0, 0, 0}; c1 = c0; } else { c0 = O[d0]; c1 = O[d0 + 1]; }
        O[d0 + 1] = mfma16(a[2 * j + 1], pf[8 * kb + kk], c1); O[d0] = mfma16(a[2 * j], pf[8 * kb + kk], c0);
        if constexpr (ls && (j & 1)) { f4 cl; if constexpr (kb == 0 && kk == 0) cl = f4{0, 0, 0, 0}; else cl = Ls; Ls = mfma16(__builtin_bit_cast(bf8, ones_u), pf[8 * kb + kk], cl); }
        if constexpr (2 * j + PD < 32) { rd(IC<2 * j + PD>{}); rd(IC<2 * j + PD + 1>{}); }
        SB(); }); };
    auto finish_o = [&]() { sfor<0, 4>([&](auto d_) { constexpr int db = decltype(d_)::v;
        hA[db] = u2{pk(O[db][0] * linv, O[db][1] * linv), pk(O[db][2] * linv, O[db][3] * linv)}; }); };
    auto write_o = [&](int hs) { const unsigned a = opq(wa0 + hs * 8192);
      sfor<0, 4>([&](auto d_) { constexpr int db = decltype(d_)::v; *(LAS u2*)(sm + (a ^ (db << 5))) = hA[db]; }); };
    sfor<0, 3>([&](auto i_) { qn[decltype(i_)::v][0] = bf8{}; qn[decltype(i_)::v][1] = bf8{}; });

#pragma clang loop unroll(disable)
    for (int it = 0; it < 2; ++it) {
      it = opqs(it);
      const int c0 = 12 * it;
      sfor<0, 6>([&](auto r_) { constexpr int r = decltype(r_)::v, ch = r >> 1, kb = r & 1;
        if constexpr (kb == 0) { bf8 n[2] = {qn[ch][0], qn[ch][1]}; if (it == 0) read_q(n, ch & 1); qc[0] = n[0]; qc[1] = n[1]; }
        k_tile(IC<kb>{}, IC<ch == 0>{});
        STAMP(6 + 4 * (c0 + r));
        bar_v<0>();
        if constexpr (r == 4) { max_half(IC<0>{}); sfor<0, 3>(exp_kk); }
        if constexpr (r == 5) { sfor<3, 8>(exp_kk); }
        if constexpr (r == 1) touch4((it ? YB : XB) + w4k, it ? v_to() : v_tq(), u4{sel(it, sO[0], sQ[0]), sel(it, sO[1], sQ[1]), sel(it, sO[2], sQ[2]), sQ[3]}, it ? 245760 : 384);
        STAMP(5 + 4 * (c0 + r));
        w_issue(c0 + r);
        STAMP(7 + 4 * (c0 + r));
        bar_l(); });
      if (it == 0) read_q(qn[0], 1);
      v_tile(IC<0>{}, IC<1>{}); max_half(IC<1>{});
      STAMP(6 + 4 * (c0 + 6));
      bar_v<0>();
      sfor<8, 16>(exp_kk);
      if (it == 0) touch4(YB + w4k, v_to(), sO, 0);
      STAMP(5 + 4 * (c0 + 6));
      w_issue(c0 + 6);
      STAMP(7 + 4 * (c0 + 6));
      bar_l();
      v_tile(IC<1>{}, IC<1>{}); linv = 1.0f / Ls[0]; finish_o();
      if (it == 1) write_o(1);
      STAMP(6 + 4 * (c0 + 7));
      bar_v<0>();
      STAMP(5 + 4 * (c0 + 7));
      w_issue(c0 + 7);
      STAMP(7 + 4 * (c0 + 7));
      bar_l();
#pragma clang loop unroll(disable)
      for (int ch = 1; ch < 3; ++ch) {
        ch = opqs(ch);
        const int c = c0 + 6 + 2 * ch;
        if (it == 0) { bf8 n[2]; read_q(n, (ch + 1) & 1); qn[1][0] = qn[2][0]; qn[1][1] = qn[2][1]; qn[2][0] = n[0]; qn[2][1] = n[1]; }
        v_tile(IC<0>{}, IC<0>{});
        STAMP(6 + 4 * c);
        bar_v<0>();
        if (it == 0) write_o((ch - 1) & 1);
        STAMP(5 + 4 * c);
        w_issue(c);
        STAMP(7 + 4 * c);
        bar_l();
        if (c == 22) w_triple();
        v_tile(IC<1>{}, IC<0>{}); finish_o();
        if (it == 1 || ch == 2) write_o((it + ch) & 1);
        STAMP(10 + 4 * c);
        bar_v<0>();
        if (c == 22) break;
        STAMP(9 + 4 * c);
        w_issue(c + 1);
        STAMP(11 + 4 * c);
        bar_l();
      }
    }
    STAMP_FLUSH();
  } else {
    __builtin_amdgcn_s_setprio(PROJ_PRIO);
    const int lane = lane_id();
    STAMP_DECL; STAMP(0);
    const int qh = w & 1, ah = w >> 1, h = lane >> 5, b16 = (lane >> 4) & 1, q4 = (lane & 15) >> 2, p = lane & 3;
    const unsigned row = 32 * qh + (lane & 31), f8 = (row >> 1) & 7;
    const unsigned qa0 = (8 * h + q4) * 128 + ((ah ^ (q4 >> 1)) << 6) + 32 * b16 + 8 * p;
    const unsigned oa0 = (8 * h + q4) * 256 + (((2 * ah) ^ q4) << 6) + 32 * b16 + 8 * p;
    const unsigned ha0 = row * 128 + ((h ^ f8) << 4) + HB;
    constexpr int PD = 6;
    const unsigned vK = v_k(lane), vV = (32 * w + (lane >> 3)) * 384 + (((lane & 7) ^ (((lane >> 4) & 3) << 1)) << 4);
    unsigned k_so = 2 * 49152, k_n = 1, k_vE = vK, k_vO = vK ^ 64; u4 k_srd = sK;
    auto kv_tile = [&]() { const unsigned lb = (k_n & 1) * KVS + w4k;
      dma4<true>(lb, k_vE, k_vO, k_srd, k_so, 3072); dma4<true>(lb + US, k_vE, k_vO, k_srd, k_so + 49152, 3072);
      ++k_n; k_so += (k_n & 1) ? 2 * 49152u : 128u - 2 * 49152u;
      if (k_n == 6 || k_n == 12 || k_n == 18) { const bool isV = k_n != 12;
        k_so = k_n == 6 ? 0 : ITEM; k_srd[0] = sel(isV, sV[0], sK[0]); k_srd[1] = sel(isV, sV[1], sK[1]); k_vE = sel(isV, vV, vK); k_vO = sel(isV, vV, vK ^ 64); } };
    auto seam = [&](bool kv) { STAMP(k_n >= 2 ? 4 * k_n - 3 : 2); bar_v<0>(); STAMP(k_n >= 2 ? 4 * k_n - 2 : 1); if (kv) kv_tile(); STAMP(k_n >= 3 ? 4 * k_n - 5 : 2); bar_l(); STAMP(k_n >= 2 ? 4 * k_n - 4 : 2); };

    {
      bf8 xf[40]; f16v Qa = {};
      auto q_run = [&](auto t0_, auto n_, unsigned lb) { constexpr int T0 = decltype(t0_)::v, N = 8 * decltype(n_)::v;
        const unsigned a0 = opq(qa0 + lb);
        bf8 a[N];
        auto rd = [&](auto i_) { constexpr int i = decltype(i_)::v; a[i] = ldtr(sm, a0 + i * 2048, a0 + i * 2048 + 512); };
        sfor<0, PD>(rd); SB();
        sfor<0, N / 2>([&](auto j_) { constexpr int i = 2 * decltype(j_)::v;
          f16v c; if constexpr (T0 == 0 && i == 0) c = f16v{}; else c = Qa;
          c = mfma32(a[i + 1], xf[8 * T0 + i + 1], c); Qa = mfma32(a[i], xf[8 * T0 + i], c);
          if constexpr (i + PD < N) { rd(IC<i + PD>{}); rd(IC<i + PD + 1>{}); }
          SB(); }); };
      bar_l();
      STAMP(105);
      { const unsigned xa = row * 256 + ((h ^ (row & 15)) << 4);
        sfor<0, 40>([&](auto s_) { constexpr int s = decltype(s_)::v; xf[s] = ld128(sm, (xa ^ ((s & 7) << 5)) + (s >= 32 ? HB : (s >> 3) * US)); }); }
      bar_l();
      STAMP(106);
      bar_l();
      STAMP(107);
#pragma clang loop unroll(disable)
      for (int gq = 0; gq < 6; ++gq) {
        gq = opqs(gq);
        q_run(IC<0>{}, IC<2>{}, XB);
        seam(gq > 0);
        q_run(IC<2>{}, IC<3>{}, YB);
        { const unsigned a = opq((ha0 ^ (h << 4)) + (gq & 1) * 8192 + 8 * h) ^ (ah << 6);
          sfor<0, 4>([&](auto i_) { constexpr int i = decltype(i_)::v;
            *(LAS u2*)(sm + (a ^ (i << 4))) = u2{pk(Qa[4 * i], Qa[4 * i + 1]), pk(Qa[4 * i + 2], Qa[4 * i + 3])}; }); }
        seam(true);
      }
    }
    {
      f16v acc[10]; bf8 ob[4];
      sfor<0, 10>([&](auto i_) { acc[decltype(i_)::v] = f16v{}; });
      const auto rsP = __builtin_amdgcn_make_buffer_rsrc(part + (size_t)(b * 16 + mem) * 81920 + (qh * 40 + 4 * ah) * 1024, 0, 81920, 0x00020000);
      auto store_t = [&](auto t_) { constexpr int T = decltype(t_)::v; const unsigned pv = lane_id() * 16;
        sfor<0, 4>([&](auto i_) { constexpr int j = decltype(i_)::v >> 1, hf = decltype(i_)::v & 1; const f16v& a = acc[2 * T + j];
          u4 u = {pk(a[8 * hf], a[8 * hf + 1]), pk(a[8 * hf + 2], a[8 * hf + 3]), pk(a[8 * hf + 4], a[8 * hf + 5]), pk(a[8 * hf + 6], a[8 * hf + 7])};
          __builtin_amdgcn_raw_buffer_store_b128(u, rsP, pv, ((4 * T + j) * 2 + hf) * 1024, PART_AUX); }); };
      auto o_run = [&](auto t0_, auto n_, unsigned lb, bool last) { constexpr int T0 = decltype(t0_)::v, N = 8 * decltype(n_)::v;
        const unsigned a0 = opq(oa0 + lb), a1 = a0 ^ 64;
        bf8 a[N];
        auto rd = [&](auto i_) { constexpr int i = decltype(i_)::v; const unsigned ad = ((i & 4) ? a1 : a0) + (i >> 3) * US + (i & 3) * 4096; a[i] = ldtr(sm, ad, ad + 1024); };
        sfor<0, PD>(rd); SB();
        sfor<0, N / 2>([&](auto j_) { constexpr int j = decltype(j_)::v, i = 2 * j, blk = 2 * T0 + (j >> 1), k = (j & 1) * 2;
          f16v c = mfma32(a[i + 1], ob[k + 1], acc[blk]); acc[blk] = mfma32(a[i], ob[k], c);
          if constexpr (i + PD < N) { rd(IC<i + PD>{}); rd(IC<i + PD + 1>{}); }
          SB();
          if constexpr ((j & 3) == 3) { if (last) store_t(IC<T0 + (j >> 2)>{}); } }); };
#pragma clang loop unroll(disable)
      for (int go = 0; go < 6; ++go) {
        go = opqs(go);
        const bool last = go == 5;
        { const unsigned ad = opq(ha0 + (go & 1) * 8192); sfor<0, 4>([&](auto k_) { constexpr int k = decltype(k_)::v; ob[k] = ld128(sm, ad ^ (k << 5)); }); }
        o_run(IC<0>{}, IC<2>{}, XB, last);
        if (!last) seam(true);
        o_run(IC<2>{}, IC<3>{}, YB, last);
        if (last) break;
        seam(go != 4);
        if (go < 3) seam(true);
        if (go == 4) STAMP(100);
      }
    }
    STAMP(102);
    STAMP_FLUSH();
  }
}

__global__ __launch_bounds__(320) void reduce_kernel(const char* __restrict__ part, char* __restrict__ out) {
  const int b = blockIdx.x >> 4, j = blockIdx.x & 15, t = threadIdx.x;
  const int pi = 5 * j + (t >> 6), r = t & 63, qh = pi / 40, L = (pi % 40) >> 2, o = pi & 3, un = r >> 4, h = (r >> 3) & 1, q8 = r & 7;
  const int nblk = 2 * L + (un >> 1), hf = un & 1;
  const char* src = part + (size_t)b * 16 * 81920 + ((qh * 20 + nblk) * 2 + hf) * 1024 + (32 * h + 8 * o + q8) * 16;
  u4 v[16];
#pragma unroll
  for (int i = 0; i < 16; ++i) v[i] = *(const u4*)(src + (size_t)i * 81920);
  float s[8] = {};
#pragma unroll
  for (int i = 0; i < 16; ++i)
#pragma unroll
    for (int e = 0; e < 4; ++e) { s[2 * e] += __uint_as_float(v[i][e] << 16); s[2 * e + 1] += __uint_as_float(v[i][e] & 0xffff0000u); }
  char* dst = out + (size_t)(b * 64 + 32 * qh + 8 * o + q8) * 1280 + (32 * nblk + 16 * hf + 4 * h) * 2;
  *(u2*)dst = u2{pk(s[0], s[1]), pk(s[2], s[3])};
  *(u2*)(dst + 16) = u2{pk(s[4], s[5]), pk(s[6], s[7])};
}

extern "C" void kernel_launch(void* const* d_in, const int* in_sizes, int n_in, void* d_out, int out_size, void* d_ws, size_t ws_size, hipStream_t stream) {
  char* part = (char*)d_ws + (1 << 20);
  fused_kernel<<<256, 512, 0, stream>>>((const char*)d_in[0], (const char*)d_in[1], (const char*)d_in[2], (const char*)d_in[3], (const char*)d_in[4], part
  );
  reduce_kernel<<<256, 320, 0, stream>>>(part, (char*)d_out);
}
```

```cpp
#include <hip/hip_runtime.h>
#include <hip/hip_bf16.h>
#include <stdint.h>
#define STAMPS
#define LITE

typedef short s4 __attribute__((ext_vector_type(4)));
typedef short s8 __attribute__((ext_vector_type(8)));
typedef __bf16 bf8 __attribute__((ext_vector_type(8)));
typedef __bf16 bf2 __attribute__((ext_vector_type(2)));
typedef float f2 __attribute__((ext_vector_type(2)));
typedef float f4 __attribute__((ext_vector_type(4)));
typedef float f16v __attribute__((ext_vector_type(16)));
typedef unsigned u2 __attribute__((ext_vector_type(2)));
typedef unsigned u4 __attribute__((ext_vector_type(4)));
typedef unsigned long long u64;
#define LAS __attribute__((address_space(3)))
#define DI __device__ __forceinline__

template <int N> struct IC { static constexpr int v = N; };
template <int A, int B, class F> DI void sfor(F&& f) { if constexpr (A < B) { f(IC<A>{}); sfor<A + 1, B>(f); } }

constexpr unsigned US = 16384, KVS = 32768, XB = 65536, YB = 98304, HB = 147456, LDS_BYTES = 163840;
constexpr unsigned ITEM = 196608;
constexpr float SC = 0.07216878364870322f * 1.4426950408889634f;

#define PROJ_PRIO 0
#define ATT_PRIO 0
#define PART_AUX 0
DI void stamp_(unsigned& s0, unsigned& s1, int i) { u64 t; __builtin_amdgcn_sched_barrier(0);
  asm volatile("s_memrealtime %0\n\ts_waitcnt lgkmcnt(0)" : "=s"(t) :: "memory");
  asm volatile("s_mov_b32 m0, %3\n\ts_nop 3\n\tv_writelane_b32 %0, %2, m0\n\ts_mov_b32 m0, %4\n\ts_nop 3\n\tv_writelane_b32 %1, %2, m0" : "+v"(s0), "+v"(s1) : "s"((unsigned)t), "s"(__builtin_amdgcn_readfirstlane(i < 64 ? i : 2)), "s"(__builtin_amdgcn_readfirstlane(i >= 64 ? i - 64 : 63)));
  __builtin_amdgcn_sched_barrier(0); }
#define STAMP(i) do { if (__builtin_constant_p(i) && ((i) < 2 || (i) >= 100)) stamp_(st0, st1, (i)); } while (0)
DI void stampc_(unsigned& s1, int i) { u64 t; asm volatile("s_memtime %0\n\ts_waitcnt lgkmcnt(0)" : "=s"(t) :: "memory");
  asm volatile("s_mov_b32 m0, %2\n\ts_nop 3\n\tv_writelane_b32 %0, %1, m0" : "+v"(s1) : "s"((unsigned)t), "s"(i - 64)); }
#define STAMP_DECL unsigned st0 = 0, st1 = 0
#define STAMP_FLUSH() do { const int l_ = lane_id(); stamps[(blockIdx.x * 8 + wave) * 128 + l_] = st0; stamps[(blockIdx.x * 8 + wave) * 128 + 64 + l_] = st1; } while (0)

#define NOPS "s_nop 4\n\t"
DI unsigned rfl(unsigned v) { return __builtin_amdgcn_readfirstlane(v); }
DI u4 rfl(u4 v) { return u4{rfl(v[0]), rfl(v[1]), rfl(v[2]), rfl(v[3])}; }
DI u4 mk_srd(const void* p, unsigned bytes) {
  const u64 a = (u64)p;
  u4 r = {(unsigned)__builtin_amdgcn_readfirstlane((unsigned)a), (unsigned)__builtin_amdgcn_readfirstlane((unsigned)(a >> 32) & 0xffffu), bytes, 0x00020000u};
  return r;
}
template <bool NT> DI void dma4(unsigned mb, unsigned v0, unsigned v1, u4 srd, unsigned so, unsigned st) {
  unsigned t;
#define DMA4_BODY(POL) \
  asm volatile(NOPS "s_mov_b32 m0, %1\n\ts_add_u32 %0, %5, %6\n\tbuffer_load_dwordx4 %2, %4, %5 offen " POL "lds\n\t" \
               "s_add_u32 m0, %1, 0x400\n\ts_nop 0\n\tbuffer_load_dwordx4 %3, %4, %0 offen " POL "lds\n\t" \
               "s_add_u32 m0, %1, 0x800\n\ts_add_u32 %0, %0, %6\n\tbuffer_load_dwordx4 %2, %4, %0 offen " POL "lds\n\t" \
               "s_add_u32 m0, %1, 0xc00\n\ts_add_u32 %0, %0, %6\n\tbuffer_load_dwordx4 %3, %4, %0 offen " POL "lds" \
               : "=&s"(t) : "s"(rfl(mb)), "v"(v0), "v"(v1), "s"(rfl(srd)), "s"(rfl(so)), "s"(rfl(st)) : "memory", "scc")
  if constexpr (NT) DMA4_BODY("nt "); else DMA4_BODY("");
#undef DMA4_BODY
}
template <int J> DI void dma1nt(unsigned mb, unsigned voff, u4 srd, unsigned so) {
  unsigned t;
  asm volatile(NOPS "s_add_u32 m0, %1, %2\n\ts_add_u32 %0, %3, %4\n\tbuffer_load_dwordx4 %5, %6, %0 offen nt lds"
               : "=&s"(t) : "s"(rfl(mb)), "i"(J * 1024), "s"(rfl(so)), "i"(J * 3072), "v"(voff), "s"(rfl(srd)) : "memory", "scc");
}
DI void dma1(unsigned m0v, unsigned voff, u4 srd, unsigned so) {
  asm volatile("s_mov_b32 m0, %0\n\ts_nop 0\n\tbuffer_load_dwordx4 %1, %2, %3 offen lds" :: "s"(m0v), "v"(voff), "s"(srd), "s"(so) : "memory");
}
DI void touch4(unsigned m0v, unsigned voff, u4 srd, unsigned so) {
  asm volatile("s_mov_b32 m0, %0\n\ts_nop 0\n\tbuffer_load_dword %1, %2, %3 offen lds" :: "s"(m0v), "v"(voff), "s"(srd), "s"(so) : "memory");
}
#define SB() __builtin_amdgcn_sched_barrier(0)
template <int N> DI void bar_v() { SB(); asm volatile("s_waitcnt vmcnt(%0) lgkmcnt(0)\n\ts_barrier" ::"i"(N) : "memory"); SB(); }
DI void bar_l() { SB(); asm volatile("s_waitcnt lgkmcnt(0)\n\ts_barrier" ::: "memory"); SB(); }

DI bf8 ld128(LAS char* sm, unsigned a) { return __builtin_bit_cast(bf8, *(LAS s8*)(sm + a)); }
DI bf8 ldtr(LAS char* sm, unsigned a0, unsigned a1) {
  s4 x = __builtin_amdgcn_ds_read_tr16_b64_v4i16((LAS s4*)(sm + a0)), y = __builtin_amdgcn_ds_read_tr16_b64_v4i16((LAS s4*)(sm + a1));
  return __builtin_bit_cast(bf8, __builtin_shufflevector(x, y, 0, 1, 2, 3, 4, 5, 6, 7));
}
DI unsigned opq(unsigned v) { asm volatile("" : "+v"(v)); return v; }
DI unsigned sel(bool c, unsigned a, unsigned b) { return c ? a : b; }
DI int opqs(int v) { v = __builtin_amdgcn_readfirstlane(v); asm volatile("" : "+s"(v)); return v; }
DI int lane_id() { unsigned z = 0; asm volatile("" : "+v"(z)); return __builtin_amdgcn_mbcnt_hi(~0u, __builtin_amdgcn_mbcnt_lo(~0u, z)); }
DI unsigned pk(float a, float b) { f2 x = {a, b}; return __builtin_bit_cast(unsigned, __builtin_convertvector(x, bf2)); }
DI f4 mfma16(bf8 a, bf8 b, f4 c) { return __builtin_amdgcn_mfma_f32_16x16x32_bf16(a, b, c, 0, 0, 0); }
DI f16v mfma32(bf8 a, bf8 b, f16v c) { return __builtin_amdgcn_mfma_f32_32x32x16_bf16(a, b, c, 0, 0, 0); }

constexpr u64 w_table() { u64 t = 0; for (int c = 0; c < 24; ++c) { const int n = c < 12 ? 2 + (c & 1) : (c == 13 || c == 16 || c == 19 || c == 21 || c == 23) ? 2 : (c == 14 || c == 17 || c == 20 || c == 22) ? 3 : 0; t |= (u64)n << (2 * c); } return t; }

__global__ __launch_bounds__(512, 2) void fused_kernel(const char* __restrict__ x, const char* __restrict__ K, const char* __restrict__ V,
                                                       const char* __restrict__ wq, const char* __restrict__ wo, char* __restrict__ part
                                                       , u64* __restrict__ stamps
) {
  __shared__ __attribute__((aligned(16))) char smem[LDS_BYTES];
  LAS char* sm = (LAS char*)smem;
  const int wave = __builtin_amdgcn_readfirstlane(threadIdx.x >> 6), w = wave & 3;
  const int xcd = blockIdx.x & 7, idx = blockIdx.x >> 3, b = idx >> 1, hp = idx & 1, h0 = xcd * 4 + hp * 2, mem = xcd * 2 + hp;
  const u4 sK = mk_srd(K + (size_t)(b * 32 + h0) * ITEM, 2 * ITEM), sV = mk_srd(V + (size_t)(b * 32 + h0) * ITEM, 2 * ITEM);
  const unsigned w4k = w * 4096u, OOB = 0x7ffffff0u;
  auto v_k = [&](int l) { return (unsigned)((32 * w + (l >> 3)) * 384 + (((l & 7) ^ (l >> 4)) << 4)); };

  if (wave < 4) {
    __builtin_amdgcn_s_setprio(ATT_PRIO);
    const int lane = lane_id();
    STAMP_DECL; STAMP(0);
    const int r16 = lane & 15, g = lane >> 4, q4 = (lane & 15) >> 2, p = lane & 3;
    const u4 sQ = mk_srd(wq + h0 * 384, 640 * 12288 - h0 * 384), sO = mk_srd(wo + (size_t)h0 * 192 * 1280, 384 * 1280);
    auto v_tq = [&]() { const unsigned l = lane_id(), tl = b * 120 + w * 30 + l; return l < 30 ? (tl / 3) * 12288 + (tl % 3) * 128 : OOB; };
    auto v_to = [&]() { const unsigned l = lane_id(), tl = b * 120 + w * 30 + l; return l < 30 ? tl * 128 : OOB; };
    unsigned w_so = 0, w_g = 0, w_v = (32 * w + (lane >> 3)) * 12288 + (((lane & 7) ^ (((lane >> 4) & 1) << 2)) << 4), w_st = 8 * 12288, w_dT = 128 * 12288, w_dG = 128u - 5u * 128 * 12288;
    u4 w_srd = sQ;
    auto w_tile = [&](unsigned lb) { dma4<false>(lb + w4k, w_v, w_v, w_srd, w_so, w_st); w_so += w_dT; };
    auto w_pair = [&]() { w_tile(XB); w_tile(XB + US); };
    auto w_triple = [&]() { w_tile(YB); w_tile(YB + US); w_tile(YB + 2 * US); w_so += w_dG;
      if (++w_g == 6) { const int l = lane_id(); w_so = 0; w_v = (16 * w + (l >> 4)) * 1280 + (((l & 15) ^ ((l >> 4) << 2)) << 4); w_st = 4 * 1280; w_dT = 256; w_dG = 64 * 1280 - 5 * 256; w_srd = sO; } };
    auto w_issue = [&](int c) { const unsigned n = (unsigned)(w_table() >> (2 * c + 2)) & 3; if (n == 2) w_pair(); else if (n == 3) w_triple(); };
    { const u4 sX = mk_srd(x + b * 81920, 81920);
      const unsigned vX = (16 * w + (lane >> 4)) * 1280 + (((lane & 15) ^ (lane >> 4)) << 4);
      const unsigned t0 = (hp ? HB : 0) + w4k;
      touch4(t0, v_tq(), sQ, 0);
      { const unsigned vt = lane < 2 ? lane * ITEM : OOB;
        if (w == 0) touch4(t0, vt, sK, 0); else if (w == 1) touch4(t0, vt, sV, 0); else if (w == 2) touch4(t0, vt, sO, 0); else touch4(t0, vt, mk_srd(part + (size_t)(b * 16 + mem) * 81920, 4096), 0); }
      for (int t = 0; t < 5; ++t) { const unsigned tt = hp ? 4 - t : t, lb = tt == 4 ? HB : tt * US;
        sfor<0, 4>([&](auto j_) { constexpr int j = decltype(j_)::v; dma1(lb + w4k + j * 1024, vX ^ (j << 6), sX, tt * 256 + j * 4 * 1280); }); }
      w_pair(); w_triple();
      STAMP(104);
      bar_v<20>();
      STAMP(105);
      bar_l();
      { const unsigned vK = v_k(lane); for (int u = 0; u < 2; ++u) dma4<true>(u * US + w4k, vK, vK ^ 64, sK, u * 49152, 3072); }
      STAMP(106);
      bar_v<8>();
      STAMP(107);
      bar_l();
      bar_l();
      w_pair();
      bar_v<8>();
      STAMP(1); }
    const unsigned ka0 = r16 * 128 + ((g ^ ((r16 >> 1) & 7)) << 4);
    const unsigned va0 = (4 * g + q4) * 128 + p * 8 + (((2 * g + (q4 >> 1)) & 3) << 5);
    const unsigned wa0 = (16 * w + r16) * 128 + (((g >> 1) ^ ((r16 >> 1) & 7)) << 4) + (g & 1) * 8 + HB;
    bf8 qc[2], qn[3][2], pf[16]; f4 S[32], O[4], Ls; u2 hA[4]; float m0, mc, alpha, linv;
    const u4 ones_u = {0x3f803f80u, 0x3f803f80u, 0x3f803f80u, 0x3f803f80u};
    auto read_q = [&](bf8* q, int hs) { const unsigned a = opq(ka0 + HB + w * 2048 + hs * 8192); q[0] = ld128(sm, a); q[1] = ld128(sm, a ^ 64); };
    auto k_tile = [&](auto kb_, auto first_) { constexpr int kb = decltype(kb_)::v, PD = 8; constexpr bool first = decltype(first_)::v;
      const unsigned a0 = opq(ka0) + kb * KVS, a1 = a0 ^ 64;
      bf8 a[32];
      auto rd = [&](auto i_) { constexpr int i = decltype(i_)::v; a[i] = ld128(sm, ((i & 1) ? a1 : a0) + (i >> 1) * 2048); };
      sfor<0, PD>(rd); SB();
      sfor<0, 16>([&](auto j_) { constexpr int j = decltype(j_)::v, blk = 16 * kb + j;
        f4 c; if constexpr (first) c = f4{0, 0, 0, 0}; else c = S[blk];
        c = mfma16(a[2 * j + 1], qc[1], c); S[blk] = mfma16(a[2 * j], qc[0], c);
        if constexpr (2 * j + PD < 32) { rd(IC<2 * j + PD>{}); rd(IC<2 * j + PD + 1>{}); }
        SB(); }); };
    auto max_half = [&](auto h_) { constexpr int h = decltype(h_)::v;
      float m = fmaxf(S[16 * h][0], S[16 * h][1]), m2 = fmaxf(S[16 * h][2], S[16 * h][3]);
      sfor<1, 16>([&](auto b_) { constexpr int i = 16 * h + decltype(b_)::v; m = fmaxf(fmaxf(m, S[i][0]), S[i][1]); m2 = fmaxf(fmaxf(m2, S[i][2]), S[i][3]); });
      m = fmaxf(m, m2);
      { auto r = __builtin_amdgcn_permlane16_swap(__float_as_uint(m), __float_as_uint(m), false, false); m = fmaxf(__uint_as_float(r[0]), __uint_as_float(r[1])); }
      { auto r = __builtin_amdgcn_permlane32_swap(__float_as_uint(m), __float_as_uint(m), false, false); m = fmaxf(__uint_as_float(r[0]), __uint_as_float(r[1])); }
      if constexpr (h == 0) m0 = m; else { m = fmaxf(m, m0); alpha = __builtin_amdgcn_exp2f((m0 - m) * SC); }
      mc = -m * SC; };
    auto exp_kk = [&](auto kk_) { constexpr int kk = decltype(kk_)::v; float e[8];
      sfor<0, 8>([&](auto j_) { constexpr int j = decltype(j_)::v; e[j] = __builtin_amdgcn_exp2f(fmaf(S[2 * kk + (j >> 2)][j & 3], SC, mc)); });
      u4 u = {pk(e[0], e[1]), pk(e[2], e[3]), pk(e[4], e[5]), pk(e[6], e[7])}; pf[kk] = __builtin_bit_cast(bf8, u); };
    auto v_tile = [&](auto kb_, auto ls_) { constexpr int kb = decltype(kb_)::v, PD = 6; constexpr bool ls = decltype(ls_)::v;
      const unsigned a0 = opq(va0) + kb * KVS;
      bf8 a[32];
      auto rd = [&](auto i_) { constexpr int i = decltype(i_)::v; const unsigned ad = (a0 ^ ((i & 3) << 5)) + (i >> 2) * 4096; a[i] = ldtr(sm, ad, ad + 2048); };
      sfor<0, PD>(rd);
      if constexpr (kb == 1) { sfor<0, 4>([&](auto d_) { constexpr int db = decltype(d_)::v; O[db] *= alpha; }); if constexpr (ls) Ls *= alpha; }
      SB();
      sfor<0, 16>([&](auto j_) { constexpr int j = decltype(j_)::v, kk = j >> 1, d0 = (j & 1) * 2;
        f4 c0, c1; if constexpr (kb == 0 && kk == 0) { c0 = f4{0, 0, 0, 0}; c1 = c0; } else { c0 = O[d0]; c1 = O[d0 + 1]; }
        O[d0 + 1] = mfma16(a[2 * j + 1], pf[8 * kb + kk], c1); O[d0] = mfma16(a[2 * j], pf[8 * kb + kk], c0);
        if constexpr (ls && (j & 1)) { f4 cl; if constexpr (kb == 0 && kk == 0) cl = f4{0, 0, 0, 0}; else cl = Ls; Ls = mfma16(__builtin_bit_cast(bf8, ones_u), pf[8 * kb + kk], cl); }
        if constexpr (2 * j + PD < 32) { rd(IC<2 * j + PD>{}); rd(IC<2 * j + PD + 1>{}); }
        SB(); }); };
    auto finish_o = [&]() { sfor<0, 4>([&](auto d_) { constexpr int db = decltype(d_)::v;
        hA[db] = u2{pk(O[db][0] * linv, O[db][1] * linv), pk(O[db][2] * linv, O[db][3] * linv)}; }); };
    auto write_o = [&](int hs) { const unsigned a = opq(wa0 + hs * 8192);
      sfor<0, 4>([&](auto d_) { constexpr int db = decltype(d_)::v; *(LAS u2*)(sm + (a ^ (db << 5))) = hA[db]; }); };
    sfor<0, 3>([&](auto i_) { qn[decltype(i_)::v][0] = bf8{}; qn[decltype(i_)::v][1] = bf8{}; });

#pragma clang loop unroll(disable)
    for (int it = 0; it < 2; ++it) {
      it = opqs(it);
      const int c0 = 12 * it;
      sfor<0, 6>([&](auto r_) { constexpr int r = decltype(r_)::v, ch = r >> 1, kb = r & 1;
        if constexpr (kb == 0) { bf8 n[2] = {qn[ch][0], qn[ch][1]}; if (it == 0) read_q(n, ch & 1); qc[0] = n[0]; qc[1] = n[1]; }
        k_tile(IC<kb>{}, IC<ch == 0>{});
        STAMP(6 + 4 * (c0 + r));
        bar_v<0>();
        if constexpr (r == 4) { max_half(IC<0>{}); sfor<0, 3>(exp_kk); }
        if constexpr (r == 5) { sfor<3, 8>(exp_kk); }
        if constexpr (r == 1) touch4((it ? YB : XB) + w4k, it ? v_to() : v_tq(), u4{sel(it, sO[0], sQ[0]), sel(it, sO[1], sQ[1]), sel(it, sO[2], sQ[2]), sQ[3]}, it ? 245760 : 384);
        STAMP(5 + 4 * (c0 + r));
        w_issue(c0 + r);
        STAMP(7 + 4 * (c0 + r));
        bar_l(); });
      if (it == 0) read_q(qn[0], 1);
      v_tile(IC<0>{}, IC<1>{}); max_half(IC<1>{});
      STAMP(6 + 4 * (c0 + 6));
      bar_v<0>();
      sfor<8, 16>(exp_kk);
      if (it == 0) touch4(YB + w4k, v_to(), sO, 0);
      STAMP(5 + 4 * (c0 + 6));
      w_issue(c0 + 6);
      STAMP(7 + 4 * (c0 + 6));
      bar_l();
      v_tile(IC<1>{}, IC<1>{}); linv = 1.0f / Ls[0]; finish_o();
      if (it == 1) write_o(1);
      STAMP(6 + 4 * (c0 + 7));
      bar_v<0>();
      STAMP(5 + 4 * (c0 + 7));
      w_issue(c0 + 7);
      STAMP(7 + 4 * (c0 + 7));
      bar_l();
#pragma clang loop unroll(disable)
      for (int ch = 1; ch < 3; ++ch) {
        ch = opqs(ch);
        const int c = c0 + 6 + 2 * ch;
        if (it == 0) { bf8 n[2]; read_q(n, (ch + 1) & 1); qn[1][0] = qn[2][0]; qn[1][1] = qn[2][1]; qn[2][0] = n[0]; qn[2][1] = n[1]; }
        v_tile(IC<0>{}, IC<0>{});
        STAMP(6 + 4 * c);
        bar_v<0>();
        if (it == 0) write_o((ch - 1) & 1);
        STAMP(5 + 4 * c);
        w_issue(c);
        STAMP(7 + 4 * c);
        bar_l();
        if (c == 22) w_triple();
        v_tile(IC<1>{}, IC<0>{}); finish_o();
        if (it == 1 || ch == 2) write_o((it + ch) & 1);
        STAMP(10 + 4 * c);
        bar_v<0>();
        if (c == 22) break;
        STAMP(9 + 4 * c);
        w_issue(c + 1);
        STAMP(11 + 4 * c);
        bar_l();
      }
    }
    STAMP_FLUSH();
  } else {
    __builtin_amdgcn_s_setprio(PROJ_PRIO);
    const int lane = lane_id();
    STAMP_DECL; STAMP(0); stampc_(st1, 110);
    const int qh = w & 1, ah = w >> 1, h = lane >> 5, b16 = (lane >> 4) & 1, q4 = (lane & 15) >> 2, p = lane & 3;
    const unsigned row = 32 * qh + (lane & 31), f8 = (row >> 1) & 7;
    const unsigned qa0 = (8 * h + q4) * 128 + ((ah ^ (q4 >> 1)) << 6) + 32 * b16 + 8 * p;
    const unsigned oa0 = (8 * h + q4) * 256 + (((2 * ah) ^ q4) << 6) + 32 * b16 + 8 * p;
    const unsigned ha0 = row * 128 + ((h ^ f8) << 4) + HB;
    constexpr int PD = 6;
    const unsigned vK = v_k(lane), vV = (32 * w + (lane >> 3)) * 384 + (((lane & 7) ^ (((lane >> 4) & 3) << 1)) << 4);
    unsigned k_so = 2 * 49152, k_n = 1, k_vE = vK, k_vO = vK ^ 64; u4 k_srd = sK;
    auto kv_tile = [&]() { const unsigned lb = (k_n & 1) * KVS + w4k;
      dma4<true>(lb, k_vE, k_vO, k_srd, k_so, 3072); dma4<true>(lb + US, k_vE, k_vO, k_srd, k_so + 49152, 3072);
      ++k_n; k_so += (k_n & 1) ? 2 * 49152u : 128u - 2 * 49152u;
      if (k_n == 6 || k_n == 12 || k_n == 18) { const bool isV = k_n != 12;
        k_so = k_n == 6 ? 0 : ITEM; k_srd[0] = sel(isV, sV[0], sK[0]); k_srd[1] = sel(isV, sV[1], sK[1]); k_vE = sel(isV, vV, vK); k_vO = sel(isV, vV, vK ^ 64); } };
    auto seam = [&](bool kv) { STAMP(k_n >= 2 ? 4 * k_n - 3 : 2); bar_v<0>(); STAMP(k_n >= 2 ? 4 * k_n - 2 : 1); if (kv) kv_tile(); STAMP(k_n >= 3 ? 4 * k_n - 5 : 2); bar_l(); STAMP(k_n >= 2 ? 4 * k_n - 4 : 2); };

    {
      bf8 xf[40]; f16v Qa = {};
      auto q_run = [&](auto t0_, auto n_, unsigned lb) { constexpr int T0 = decltype(t0_)::v, N = 8 * decltype(n_)::v;
        const unsigned a0 = opq(qa0 + lb);
        bf8 a[N];
        auto rd = [&](auto i_) { constexpr int i = decltype(i_)::v; a[i] = ldtr(sm, a0 + i * 2048, a0 + i * 2048 + 512); };
        sfor<0, PD>(rd); SB();
        sfor<0, N / 2>([&](auto j_) { constexpr int i = 2 * decltype(j_)::v;
          f16v c; if constexpr (T0 == 0 && i == 0) c = f16v{}; else c = Qa;
          c = mfma32(a[i + 1], xf[8 * T0 + i + 1], c); Qa = mfma32(a[i], xf[8 * T0 + i], c);
          if constexpr (i + PD < N) { rd(IC<i + PD>{}); rd(IC<i + PD + 1>{}); }
          SB(); }); };
      bar_l();
      STAMP(105);
      { const unsigned xa = row * 256 + ((h ^ (row & 15)) << 4);
        sfor<0, 40>([&](auto s_) { constexpr int s = decltype(s_)::v; xf[s] = ld128(sm, (xa ^ ((s & 7) << 5)) + (s >= 32 ? HB : (s >> 3) * US)); }); }
      bar_l();
      STAMP(106);
      bar_l();
      STAMP(107);
#pragma clang loop unroll(disable)
      for (int gq = 0; gq < 6; ++gq) {
        gq = opqs(gq);
        q_run(IC<0>{}, IC<2>{}, XB);
        seam(gq > 0);
        q_run(IC<2>{}, IC<3>{}, YB);
        { const unsigned a = opq((ha0 ^ (h << 4)) + (gq & 1) * 8192 + 8 * h) ^ (ah << 6);
          sfor<0, 4>([&](auto i_) { constexpr int i = decltype(i_)::v;
            *(LAS u2*)(sm + (a ^ (i << 4))) = u2{pk(Qa[4 * i], Qa[4 * i + 1]), pk(Qa[4 * i + 2], Qa[4 * i + 3])}; }); }
        seam(true);
      }
    }
    {
      f16v acc[10]; bf8 ob[4];
      sfor<0, 10>([&](auto i_) { acc[decltype(i_)::v] = f16v{}; });
      const auto rsP = __builtin_amdgcn_make_buffer_rsrc(part + (size_t)(b * 16 + mem) * 81920 + (qh * 40 + 4 * ah) * 1024, 0, 81920, 0x00020000);
      auto store_t = [&](auto t_) { constexpr int T = decltype(t_)::v; const unsigned pv = lane_id() * 16;
        sfor<0, 4>([&](auto i_) { constexpr int j = decltype(i_)::v >> 1, hf = decltype(i_)::v & 1; const f16v& a = acc[2 * T + j];
          u4 u = {pk(a[8 * hf], a[8 * hf + 1]), pk(a[8 * hf + 2], a[8 * hf + 3]), pk(a[8 * hf + 4], a[8 * hf + 5]), pk(a[8 * hf + 6], a[8 * hf + 7])};
          __builtin_amdgcn_raw_buffer_store_b128(u, rsP, pv, ((4 * T + j) * 2 + hf) * 1024, PART_AUX); }); };
      auto o_run = [&](auto t0_, auto n_, unsigned lb, bool last) { constexpr int T0 = decltype(t0_)::v, N = 8 * decltype(n_)::v;
        const unsigned a0 = opq(oa0 + lb), a1 = a0 ^ 64;
        bf8 a[N];
        auto rd = [&](auto i_) { constexpr int i = decltype(i_)::v; const unsigned ad = ((i & 4) ? a1 : a0) + (i >> 3) * US + (i & 3) * 4096; a[i] = ldtr(sm, ad, ad + 1024); };
        sfor<0, PD>(rd); SB();
        sfor<0, N / 2>([&](auto j_) { constexpr int j = decltype(j_)::v, i = 2 * j, blk = 2 * T0 + (j >> 1), k = (j & 1) * 2;
          f16v c = mfma32(a[i + 1], ob[k + 1], acc[blk]); acc[blk] = mfma32(a[i], ob[k], c);
          if constexpr (i + PD < N) { rd(IC<i + PD>{}); rd(IC<i + PD + 1>{}); }
          SB();
          if constexpr ((j & 3) == 3) { if (last) store_t(IC<T0 + (j >> 2)>{}); } }); };
#pragma clang loop unroll(disable)
      for (int go = 0; go < 6; ++go) {
        go = opqs(go);
        const bool last = go == 5;
        { const unsigned ad = opq(ha0 + (go & 1) * 8192); sfor<0, 4>([&](auto k_) { constexpr int k = decltype(k_)::v; ob[k] = ld128(sm, ad ^ (k << 5)); }); }
        o_run(IC<0>{}, IC<2>{}, XB, last);
        if (!last) seam(true);
        o_run(IC<2>{}, IC<3>{}, YB, last);
        if (last) break;
        seam(go != 4);
        if (go < 3) seam(true);
        if (go == 4) STAMP(100);
      }
    }
    STAMP(102);
    asm volatile("s_waitcnt vmcnt(0)" ::: "memory"); STAMP(103); stampc_(st1, 111);
    STAMP_FLUSH();
  }
}

__global__ void spin_kernel(const u64* st, int mode) {
  unsigned mn = 0xffffffffu, mx = 0, mp = 0, ma = 0, rs = 0xffffffffu, re = 0; const unsigned ref = (unsigned)st[4 * 128];
  for (int i = 0; i < 256; ++i) { const u64* s = st + (i * 8 + 4) * 128; unsigned a = (unsigned)s[0] - ref + 100000, e = (unsigned)s[103] - ref + 100000, p = (unsigned)s[107] - ref + 100000, q = (unsigned)s[100] - ref + 100000;
    mn = a < mn ? a : mn; mx = e > mx ? e : mx; mp = p > mp ? p : mp; ma = q > ma ? q : ma;
    unsigned r0 = (unsigned)st[300000 + 2 * i] - ref + 100000, r1 = (unsigned)st[300001 + 2 * i] - ref + 100000; rs = r0 < rs ? r0 : rs; re = r1 > re ? r1 : re; }
  const u64* s = st + (37 * 8 + 4) * 128; float clk = (float)((unsigned)s[111] - (unsigned)s[110]) / (float)((unsigned)s[103] - (unsigned)s[0]);
  u64 T = mode == 0 ? (u64)(mx - mn) * 20 : mode == 1 ? (u64)(clk * 5000.f) : mode == 2 ? (u64)(mp - mn) * 100 : mode == 3 ? (u64)(ma - mn) * 20 : mode == 4 ? (u64)(rs - mx) * 100 : (u64)(re - rs) * 100;
  if (T > 400000) T = 400000;
  u64 t0 = __builtin_amdgcn_s_memrealtime(); while (__builtin_amdgcn_s_memrealtime() - t0 < T) __builtin_amdgcn_s_sleep(8);
}
__global__ __launch_bounds__(320) void reduce_kernel(const char* __restrict__ part, char* __restrict__ out, u64* st) {
  u64 t0_ = __builtin_amdgcn_s_memrealtime();
  const int b = blockIdx.x >> 4, j = blockIdx.x & 15, t = threadIdx.x;
  const int pi = 5 * j + (t >> 6), r = t & 63, qh = pi / 40, L = (pi % 40) >> 2, o = pi & 3, un = r >> 4, h = (r >> 3) & 1, q8 = r & 7;
  const int nblk = 2 * L + (un >> 1), hf = un & 1;
  const char* src = part + (size_t)b * 16 * 81920 + ((qh * 20 + nblk) * 2 + hf) * 1024 + (32 * h + 8 * o + q8) * 16;
  u4 v[16];
#pragma unroll
  for (int i = 0; i < 16; ++i) v[i] = *(const u4*)(src + (size_t)i * 81920);
  float s[8] = {};
#pragma unroll
  for (int i = 0; i < 16; ++i)
#pragma unroll
    for (int e = 0; e < 4; ++e) { s[2 * e] += __uint_as_float(v[i][e] << 16); s[2 * e + 1] += __uint_as_float(v[i][e] & 0xffff0000u); }
  char* dst = out + (size_t)(b * 64 + 32 * qh + 8 * o + q8) * 1280 + (32 * nblk + 16 * hf + 4 * h) * 2;
  *(u2*)dst = u2{pk(s[0], s[1]), pk(s[2], s[3])};
  *(u2*)(dst + 16) = u2{pk(s[4], s[5]), pk(s[6], s[7])};
  asm volatile("s_waitcnt vmcnt(0)" ::: "memory");
  if (t == 0) { st[300000 + 2 * blockIdx.x] = (unsigned)t0_; st[300001 + 2 * blockIdx.x] = (unsigned)__builtin_amdgcn_s_memrealtime(); }
}

extern "C" void kernel_launch(void* const* d_in, const int* in_sizes, int n_in, void* d_out, int out_size, void* d_ws, size_t ws_size, hipStream_t stream) {
  char* part = (char*)d_ws + (1 << 20);
  fused_kernel<<<256, 512, 0, stream>>>((const char*)d_in[0], (const char*)d_in[1], (const char*)d_in[2], (const char*)d_in[3], (const char*)d_in[4], part
                                        , (u64*)((char*)d_ws + (64 << 20))
  );
  reduce_kernel<<<256, 320, 0, stream>>>(part, (char*)d_out, (u64*)((char*)d_ws + (64 << 20)));
  spin_kernel<<<1, 64, 0, stream>>>((const u64*)((char*)d_ws + (64 << 20)), 5);
}
```

```cpp
#include <hip/hip_runtime.h>
#include <hip/hip_bf16.h>
#include <stdint.h>
#define STAMPS
#define LITE

typedef short s4 __attribute__((ext_vector_type(4)));
typedef short s8 __attribute__((ext_vector_type(8)));
typedef __bf16 bf8 __attribute__((ext_vector_type(8)));
typedef __bf16 bf2 __attribute__((ext_vector_type(2)));
typedef float f2 __attribute__((ext_vector_type(2)));
typedef float f4 __attribute__((ext_vector_type(4)));
typedef float f16v __attribute__((ext_vector_type(16)));
typedef unsigned u2 __attribute__((ext_vector_type(2)));
typedef unsigned u4 __attribute__((ext_vector_type(4)));
typedef unsigned long long u64;
#define LAS __attribute__((address_space(3)))
#define DI __device__ __forceinline__

template <int N> struct IC { static constexpr int v = N; };
template <int A, int B, class F> DI void sfor(F&& f) { if constexpr (A < B) { f(IC<A>{}); sfor<A + 1, B>(f); } }

constexpr unsigned US = 16384, KVS = 32768, XB = 65536, YB = 98304, HB = 147456, LDS_BYTES = 163840;
constexpr unsigned ITEM = 196608;
constexpr float SC = 0.07216878364870322f * 1.4426950408889634f;

#define PROJ_PRIO 0
#define ATT_PRIO 0
#define PART_AUX 0
DI void stamp_(unsigned& s0, unsigned& s1, int i) { u64 t; __builtin_amdgcn_sched_barrier(0);
  asm volatile("s_memrealtime %0\n\ts_waitcnt lgkmcnt(0)" : "=s"(t) :: "memory");
  asm volatile("s_mov_b32 m0, %3\n\ts_nop 3\n\tv_writelane_b32 %0, %2, m0\n\ts_mov_b32 m0, %4\n\ts_nop 3\n\tv_writelane_b32 %1, %2, m0" : "+v"(s0), "+v"(s1) : "s"((unsigned)t), "s"(__builtin_amdgcn_readfirstlane(i < 64 ? i : 2)), "s"(__builtin_amdgcn_readfirstlane(i >= 64 ? i - 64 : 63)));
  __builtin_amdgcn_sched_barrier(0); }
#define STAMP(i) do { if (__builtin_constant_p(i) && ((i) < 2 || (i) >= 100)) stamp_(st0, st1, (i)); } while (0)
DI void stampc_(unsigned& s1, int i) { u64 t; asm volatile("s_memtime %0\n\ts_waitcnt lgkmcnt(0)" : "=s"(t) :: "memory");
  asm volatile("s_mov_b32 m0, %2\n\ts_nop 3\n\tv_writelane_b32 %0, %1, m0" : "+v"(s1) : "s"((unsigned)t), "s"(i - 64)); }
#define STAMP_DECL unsigned st0 = 0, st1 = 0
#define STAMP_FLUSH() do { const int l_ = lane_id(); stamps[(blockIdx.x * 8 + wave) * 128 + l_] = st0; stamps[(blockIdx.x * 8 + wave) * 128 + 64 + l_] = st1; } while (0)

#define NOPS "s_nop 4\n\t"
DI unsigned rfl(unsigned v) { return __builtin_amdgcn_readfirstlane(v); }
DI u4 rfl(u4 v) { return u4{rfl(v[0]), rfl(v[1]), rfl(v[2]), rfl(v[3])}; }
DI u4 mk_srd(const void* p, unsigned bytes) {
  const u64 a = (u64)p;
  u4 r = {(unsigned)__builtin_amdgcn_readfirstlane((unsigned)a), (unsigned)__builtin_amdgcn_readfirstlane((unsigned)(a >> 32) & 0xffffu), bytes, 0x00020000u};
  return r;
}
template <bool NT> DI void dma4(unsigned mb, unsigned v0, unsigned v1, u4 srd, unsigned so, unsigned st) {
  unsigned t;
#define DMA4_BODY(POL) \
  asm volatile(NOPS "s_mov_b32 m0, %1\n\ts_add_u32 %0, %5, %6\n\tbuffer_load_dwordx4 %2, %4, %5 offen " POL "lds\n\t" \
               "s_add_u32 m0, %1, 0x400\n\ts_nop 0\n\tbuffer_load_dwordx4 %3, %4, %0 offen " POL "lds\n\t" \
               "s_add_u32 m0, %1, 0x800\n\ts_add_u32 %0, %0, %6\n\tbuffer_load_dwordx4 %2, %4, %0 offen " POL "lds\n\t" \
               "s_add_u32 m0, %1, 0xc00\n\ts_add_u32 %0, %0, %6\n\tbuffer_load_dwordx4 %3, %4, %0 offen " POL "lds" \
               : "=&s"(t) : "s"(rfl(mb)), "v"(v0), "v"(v1), "s"(rfl(srd)), "s"(rfl(so)), "s"(rfl(st)) : "memory", "scc")
  if constexpr (NT) DMA4_BODY("nt "); else DMA4_BODY("");
#undef DMA4_BODY
}
template <int J> DI void dma1nt(unsigned mb, unsigned voff, u4 srd, unsigned so) {
  unsigned t;
  asm volatile(NOPS "s_add_u32 m0, %1, %2\n\ts_add_u32 %0, %3, %4\n\tbuffer_load_dwordx4 %5, %6, %0 offen nt lds"
               : "=&s"(t) : "s"(rfl(mb)), "i"(J * 1024), "s"(rfl(so)), "i"(J * 3072), "v"(voff), "s"(rfl(srd)) : "memory", "scc");
}
DI void dma1(unsigned m0v, unsigned voff, u4 srd, unsigned so) {
  asm volatile("s_mov_b32 m0, %0\n\ts_nop 0\n\tbuffer_load_dwordx4 %1, %2, %3 offen lds" :: "s"(m0v), "v"(voff), "s"(srd), "s"(so) : "memory");
}
DI void touch4(unsigned m0v, unsigned voff, u4 srd, unsigned so) {
  asm volatile("s_mov_b32 m0, %0\n\ts_nop 0\n\tbuffer_load_dword %1, %2, %3 offen lds" :: "s"(m0v), "v"(voff), "s"(srd), "s"(so) : "memory");
}
#define SB() __builtin_amdgcn_sched_barrier(0)
template <int N> DI void bar_v() { SB(); asm volatile("s_waitcnt vmcnt(%0) lgkmcnt(0)\n\ts_barrier" ::"i"(N) : "memory"); SB(); }
DI void bar_l() { SB(); asm volatile("s_waitcnt lgkmcnt(0)\n\ts_barrier" ::: "memory"); SB(); }

DI bf8 ld128(LAS char* sm, unsigned a) { return __builtin_bit_cast(bf8, *(LAS s8*)(sm + a)); }
DI bf8 ldtr(LAS char* sm, unsigned a0, unsigned a1) {
  s4 x = __builtin_amdgcn_ds_read_tr16_b64_v4i16((LAS s4*)(sm + a0)), y = __builtin_amdgcn_ds_read_tr16_b64_v4i16((LAS s4*)(sm + a1));
  return __builtin_bit_cast(bf8, __builtin_shufflevector(x, y, 0, 1, 2, 3, 4, 5, 6, 7));
}
DI unsigned opq(unsigned v) { asm volatile("" : "+v"(v)); return v; }
DI unsigned sel(bool c, unsigned a, unsigned b) { return c ? a : b; }
DI int opqs(int v) { v = __builtin_amdgcn_readfirstlane(v); asm volatile("" : "+s"(v)); return v; }
DI int lane_id() { unsigned z = 0; asm volatile("" : "+v"(z)); return __builtin_amdgcn_mbcnt_hi(~0u, __builtin_amdgcn_mbcnt_lo(~0u, z)); }
DI unsigned pk(float a, float b) { f2 x = {a, b}; return __builtin_bit_cast(unsigned, __builtin_convertvector(x, bf2)); }
DI f4 mfma16(bf8 a, bf8 b, f4 c) { return __builtin_amdgcn_mfma_f32_16x16x32_bf16(a, b, c, 0, 0, 0); }
DI f16v mfma32(bf8 a, bf8 b, f16v c) { return __builtin_amdgcn_mfma_f32_32x32x16_bf16(a, b, c, 0, 0, 0); }

constexpr u64 w_table() { u64 t = 0; for (int c = 0; c < 24; ++c) { const int n = c < 12 ? 2 + (c & 1) : (c == 13 || c == 16 || c == 19 || c == 21 || c == 23) ? 2 : (c == 14 || c == 17 || c == 20 || c == 22) ? 3 : 0; t |= (u64)n << (2 * c); } return t; }

__global__ __launch_bounds__(512, 2) void fused_kernel(const char* __restrict__ x, const char* __restrict__ K, const char* __restrict__ V,
                                                       const char* __restrict__ wq, const char* __restrict__ wo, char* __restrict__ part
                                                       , u64* __restrict__ stamps
) {
  __shared__ __attribute__((aligned(16))) char smem[LDS_BYTES];
  LAS char* sm = (LAS char*)smem;
  const int wave = __builtin_amdgcn_readfirstlane(threadIdx.x >> 6), w = wave & 3;
  const int xcd = blockIdx.x & 7, idx = blockIdx.x >> 3, b = idx >> 1, hp = idx & 1, h0 = xcd * 4 + hp * 2, mem = xcd * 2 + hp;
  const u4 sK = mk_srd(K + (size_t)(b * 32 + h0) * ITEM, 2 * ITEM), sV = mk_srd(V + (size_t)(b * 32 + h0) * ITEM, 2 * ITEM);
  const unsigned w4k = w * 4096u, OOB = 0x7ffffff0u;
  auto v_k = [&](int l) { return (unsigned)((32 * w + (l >> 3)) * 384 + (((l & 7) ^ (l >> 4)) << 4)); };

  if (wave < 4) {
    __builtin_amdgcn_s_setprio(ATT_PRIO);
    const int lane = lane_id();
    STAMP_DECL; STAMP(0);
    const int r16 = lane & 15, g = lane >> 4, q4 = (lane & 15) >> 2, p = lane & 3;
    const u4 sQ = mk_srd(wq + h0 * 384, 640 * 12288 - h0 * 384), sO = mk_srd(wo + (size_t)h0 * 192 * 1280, 384 * 1280);
    auto v_tq = [&]() { const unsigned l = lane_id(), tl = b * 120 + w * 30 + l; return l < 30 ? (tl / 3) * 12288 + (tl % 3) * 128 : OOB; };
    auto v_to = [&]() { const unsigned l = lane_id(), tl = b * 120 + w * 30 + l; return l < 30 ? tl * 128 : OOB; };
    unsigned w_so = 0, w_g = 0, w_v = (32 * w + (lane >> 3)) * 12288 + (((lane & 7) ^ (((lane >> 4) & 1) << 2)) << 4), w_st = 8 * 12288, w_dT = 128 * 12288, w_dG = 128u - 5u * 128 * 12288;
    u4 w_srd = sQ;
    auto w_tile = [&](unsigned lb) { dma4<false>(lb + w4k, w_v, w_v, w_srd, w_so, w_st); w_so += w_dT; };
    auto w_pair = [&]() { w_tile(XB); w_tile(XB + US); };
    auto w_triple = [&]() { w_tile(YB); w_tile(YB + US); w_tile(YB + 2 * US); w_so += w_dG;
      if (++w_g == 6) { const int l = lane_id(); w_so = 0; w_v = (16 * w + (l >> 4)) * 1280 + (((l & 15) ^ ((l >> 4) << 2)) << 4); w_st = 4 * 1280; w_dT = 256; w_dG = 64 * 1280 - 5 * 256; w_srd = sO; } };
    auto w_issue = [&](int c) { const unsigned n = (unsigned)(w_table() >> (2 * c + 2)) & 3; if (n == 2) w_pair(); else if (n == 3) w_triple(); };
    { const u4 sX = mk_srd(x + b * 81920, 81920);
      const unsigned vX = (16 * w + (lane >> 4)) * 1280 + (((lane & 15) ^ (lane >> 4)) << 4);
      const unsigned t0 = (hp ? HB : 0) + w4k;
      touch4(t0, v_tq(), sQ, 0);
      { const unsigned vt = lane < 2 ? lane * ITEM : OOB;
        if (w == 0) touch4(t0, vt, sK, 0); else if (w == 1) touch4(t0, vt, sV, 0); else if (w == 2) touch4(t0, vt, sO, 0); else touch4(t0, vt, mk_srd(part + (size_t)(b * 16 + mem) * 81920, 4096), 0); }
      for (int t = 0; t < 5; ++t) { const unsigned tt = hp ? 4 - t : t, lb = tt == 4 ? HB : tt * US;
        sfor<0, 4>([&](auto j_) { constexpr int j = decltype(j_)::v; dma1(lb + w4k + j * 1024, vX ^ (j << 6), sX, tt * 256 + j * 4 * 1280); }); }
      w_pair(); w_triple();
      STAMP(104);
      bar_v<20>();
      STAMP(105);
      bar_l();
      { const unsigned vK = v_k(lane); for (int u = 0; u < 2; ++u) dma4<true>(u * US + w4k, vK, vK ^ 64, sK, u * 49152, 3072); }
      STAMP(106);
      bar_v<8>();
      STAMP(107);
      bar_l();
      bar_l();
      w_pair();
      bar_v<8>();
      STAMP(1); }
    const unsigned ka0 = r16 * 128 + ((g ^ ((r16 >> 1) & 7)) << 4);
    const unsigned va0 = (4 * g + q4) * 128 + p * 8 + (((2 * g + (q4 >> 1)) & 3) << 5);
    const unsigned wa0 = (16 * w + r16) * 128 + (((g >> 1) ^ ((r16 >> 1) & 7)) << 4) + (g & 1) * 8 + HB;
    bf8 qc[2], qn[3][2], pf[16]; f4 S[32], O[4], Ls; u2 hA[4]; float m0, mc, alpha, linv;
    const u4 ones_u = {0x3f803f80u, 0x3f803f80u, 0x3f803f80u, 0x3f803f80u};
    auto read_q = [&](bf8* q, int hs) { const unsigned a = opq(ka0 + HB + w * 2048 + hs * 8192); q[0] = ld128(sm, a); q[1] = ld128(sm, a ^ 64); };
    auto k_tile = [&](auto kb_, auto first_) { constexpr int kb = decltype(kb_)::v, PD = 8; constexpr bool first = decltype(first_)::v;
      const unsigned a0 = opq(ka0) + kb * KVS, a1 = a0 ^ 64;
      bf8 a[32];
      auto rd = [&](auto i_) { constexpr int i = decltype(i_)::v; a[i] = ld128(sm, ((i & 1) ? a1 : a0) + (i >> 1) * 2048); };
      sfor<0, PD>(rd); SB();
      sfor<0, 16>([&](auto j_) { constexpr int j = decltype(j_)::v, blk = 16 * kb + j;
        f4 c; if constexpr (first) c = f4{0, 0, 0, 0}; else c = S[blk];
        c = mfma16(a[2 * j + 1], qc[1], c); S[blk] = mfma16(a[2 * j], qc[0], c);
        if constexpr (2 * j + PD < 32) { rd(IC<2 * j + PD>{}); rd(IC<2 * j + PD + 1>{}); }
        SB(); }); };
    auto max_half = [&](auto h_) { constexpr int h = decltype(h_)::v;
      float m = fmaxf(S[16 * h][0], S[16 * h][1]), m2 = fmaxf(S[16 * h][2], S[16 * h][3]);
      sfor<1, 16>([&](auto b_) { constexpr int i = 16 * h + decltype(b_)::v; m = fmaxf(fmaxf(m, S[i][0]), S[i][1]); m2 = fmaxf(fmaxf(m2, S[i][2]), S[i][3]); });
      m = fmaxf(m, m2);
      { auto r = __builtin_amdgcn_permlane16_swap(__float_as_uint(m), __float_as_uint(m), false, false); m = fmaxf(__uint_as_float(r[0]), __uint_as_float(r[1])); }
      { auto r = __builtin_amdgcn_permlane32_swap(__float_as_uint(m), __float_as_uint(m), false, false); m = fmaxf(__uint_as_float(r[0]), __uint_as_float(r[1])); }
      if constexpr (h == 0) m0 = m; else { m = fmaxf(m, m0); alpha = __builtin_amdgcn_exp2f((m0 - m) * SC); }
      mc = -m * SC; };
    auto exp_kk = [&](auto kk_) { constexpr int kk = decltype(kk_)::v; float e[8];
      sfor<0, 8>([&](auto j_) { constexpr int j = decltype(j_)::v; e[j] = __builtin_amdgcn_exp2f(fmaf(S[2 * kk + (j >> 2)][j & 3], SC, mc)); });
      u4 u = {pk(e[0], e[1]), pk(e[2], e[3]), pk(e[4], e[5]), pk(e[6], e[7])}; pf[kk] = __builtin_bit_cast(bf8, u); };
    auto v_tile = [&](auto kb_, auto ls_) { constexpr int kb = decltype(kb_)::v, PD = 6; constexpr bool ls = decltype(ls_)::v;
      const unsigned a0 = opq(va0) + kb * KVS;
      bf8 a[32];
      auto rd = [&](auto i_) { constexpr int i = decltype(i_)::v; const unsigned ad = (a0 ^ ((i & 3) << 5)) + (i >> 2) * 4096; a[i] = ldtr(sm, ad, ad + 2048); };
      sfor<0, PD>(rd);
      if constexpr (kb == 1) { sfor<0, 4>([&](auto d_) { constexpr int db = decltype(d_)::v; O[db] *= alpha; }); if constexpr (ls) Ls *= alpha; }
      SB();
      sfor<0, 16>([&](auto j_) { constexpr int j = decltype(j_)::v, kk = j >> 1, d0 = (j & 1) * 2;
        f4 c0, c1; if constexpr (kb == 0 && kk == 0) { c0 = f4{0, 0, 0, 0}; c1 = c0; } else { c0 = O[d0]; c1 = O[d0 + 1]; }
        O[d0 + 1] = mfma16(a[2 * j + 1], pf[8 * kb + kk], c1); O[d0] = mfma16(a[2 * j], pf[8 * kb + kk], c0);
        if constexpr (ls && (j & 1)) { f4 cl; if constexpr (kb == 0 && kk == 0) cl = f4{0, 0, 0, 0}; else cl = Ls; Ls = mfma16(__builtin_bit_cast(bf8, ones_u), pf[8 * kb + kk], cl); }
        if constexpr (2 * j + PD < 32) { rd(IC<2 * j + PD>{}); rd(IC<2 * j + PD + 1>{}); }
        SB(); }); };
    auto finish_o = [&]() { sfor<0, 4>([&](auto d_) { constexpr int db = decltype(d_)::v;
        hA[db] = u2{pk(O[db][0] * linv, O[db][1] * linv), pk(O[db][2] * linv, O[db][3] * linv)}; }); };
    auto write_o = [&](int hs) { const unsigned a = opq(wa0 + hs * 8192);
      sfor<0, 4>([&](auto d_) { constexpr int db = decltype(d_)::v; *(LAS u2*)(sm + (a ^ (db << 5))) = hA[db]; }); };
    sfor<0, 3>([&](auto i_) { qn[decltype(i_)::v][0] = bf8{}; qn[decltype(i_)::v][1] = bf8{}; });

#pragma clang loop unroll(disable)
    for (int it = 0; it < 2; ++it) {
      it = opqs(it);
      const int c0 = 12 * it;
      sfor<0, 6>([&](auto r_) { constexpr int r = decltype(r_)::v, ch = r >> 1, kb = r & 1;
        if constexpr (kb == 0) { bf8 n[2] = {qn[ch][0], qn[ch][1]}; if (it == 0) read_q(n, ch & 1); qc[0] = n[0]; qc[1] = n[1]; }
        k_tile(IC<kb>{}, IC<ch == 0>{});
        STAMP(6 + 4 * (c0 + r));
        bar_v<0>();
        if constexpr (r == 4) { max_half(IC<0>{}); sfor<0, 3>(exp_kk); }
        if constexpr (r == 5) { sfor<3, 8>(exp_kk); }
        if constexpr (r == 1) touch4((it ? YB : XB) + w4k, it ? v_to() : v_tq(), u4{sel(it, sO[0], sQ[0]), sel(it, sO[1], sQ[1]), sel(it, sO[2], sQ[2]), sQ[3]}, it ? 245760 : 384);
        STAMP(5 + 4 * (c0 + r));
        w_issue(c0 + r);
        STAMP(7 + 4 * (c0 + r));
        bar_l(); });
      if (it == 0) read_q(qn[0], 1);
      v_tile(IC<0>{}, IC<1>{}); max_half(IC<1>{});
      STAMP(6 + 4 * (c0 + 6));
      bar_v<0>();
      sfor<8, 16>(exp_kk);
      if (it == 0) touch4(YB + w4k, v_to(), sO, 0);
      STAMP(5 + 4 * (c0 + 6));
      w_issue(c0 + 6);
      STAMP(7 + 4 * (c0 + 6));
      bar_l();
      v_tile(IC<1>{}, IC<1>{}); linv = 1.0f / Ls[0]; finish_o();
      if (it == 1) write_o(1);
      STAMP(6 + 4 * (c0 + 7));
      bar_v<0>();
      STAMP(5 + 4 * (c0 + 7));
      w_issue(c0 + 7);
      STAMP(7 + 4 * (c0 + 7));
      bar_l();
#pragma clang loop unroll(disable)
      for (int ch = 1; ch < 3; ++ch) {
        ch = opqs(ch);
        const int c = c0 + 6 + 2 * ch;
        if (it == 0) { bf8 n[2]; read_q(n, (ch + 1) & 1); qn[1][0] = qn[2][0]; qn[1][1] = qn[2][1]; qn[2][0] = n[0]; qn[2][1] = n[1]; }
        v_tile(IC<0>{}, IC<0>{});
        STAMP(6 + 4 * c);
        bar_v<0>();
        if (it == 0) write_o((ch - 1) & 1);
        STAMP(5 + 4 * c);
        w_issue(c);
        STAMP(7 + 4 * c);
        bar_l();
        if (c == 22) w_triple();
        v_tile(IC<1>{}, IC<0>{}); finish_o();
        if (it == 1 || ch == 2) write_o((it + ch) & 1);
        STAMP(10 + 4 * c);
        bar_v<0>();
        if (c == 22) break;
        STAMP(9 + 4 * c);
        w_issue(c + 1);
        STAMP(11 + 4 * c);
        bar_l();
      }
    }
    STAMP_FLUSH();
  } else {
    __builtin_amdgcn_s_setprio(PROJ_PRIO);
    const int lane = lane_id();
    STAMP_DECL; STAMP(0); stampc_(st1, 110);
    const int qh = w & 1, ah = w >> 1, h = lane >> 5, b16 = (lane >> 4) & 1, q4 = (lane & 15) >> 2, p = lane & 3;
    const unsigned row = 32 * qh + (lane & 31), f8 = (row >> 1) & 7;
    const unsigned qa0 = (8 * h + q4) * 128 + ((ah ^ (q4 >> 1)) << 6) + 32 * b16 + 8 * p;
    const unsigned oa0 = (8 * h + q4) * 256 + (((2 * ah) ^ q4) << 6) + 32 * b16 + 8 * p;
    const unsigned ha0 = row * 128 + ((h ^ f8) << 4) + HB;
    constexpr int PD = 6;
    const unsigned vK = v_k(lane), vV = (32 * w + (lane >> 3)) * 384 + (((lane & 7) ^ (((lane >> 4) & 3) << 1)) << 4);
    unsigned k_so = 2 * 49152, k_n = 1, k_vE = vK, k_vO = vK ^ 64; u4 k_srd = sK;
    auto kv_tile = [&]() { const unsigned lb = (k_n & 1) * KVS + w4k;
      dma4<true>(lb, k_vE, k_vO, k_srd, k_so, 3072); dma4<true>(lb + US, k_vE, k_vO, k_srd, k_so + 49152, 3072);
      ++k_n; k_so += (k_n & 1) ? 2 * 49152u : 128u - 2 * 49152u;
      if (k_n == 6 || k_n == 12 || k_n == 18) { const bool isV = k_n != 12;
        k_so = k_n == 6 ? 0 : ITEM; k_srd[0] = sel(isV, sV[0], sK[0]); k_srd[1] = sel(isV, sV[1], sK[1]); k_vE = sel(isV, vV, vK); k_vO = sel(isV, vV, vK ^ 64); } };
    auto seam = [&](bool kv) { STAMP(k_n >= 2 ? 4 * k_n - 3 : 2); bar_v<0>(); STAMP(k_n >= 2 ? 4 * k_n - 2 : 1); if (kv) kv_tile(); STAMP(k_n >= 3 ? 4 * k_n - 5 : 2); bar_l(); STAMP(k_n >= 2 ? 4 * k_n - 4 : 2); };

    {
      bf8 xf[40]; f16v Qa = {};
      auto q_run = [&](auto t0_, auto n_, unsigned lb) { constexpr int T0 = decltype(t0_)::v, N = 8 * decltype(n_)::v;
        const unsigned a0 = opq(qa0 + lb);
        bf8 a[N];
        auto rd = [&](auto i_) { constexpr int i = decltype(i_)::v; a[i] = ldtr(sm, a0 + i * 2048, a0 + i * 2048 + 512); };
        sfor<0, PD>(rd); SB();
        sfor<0, N / 2>([&](auto j_) { constexpr int i = 2 * decltype(j_)::v;
          f16v c; if constexpr (T0 == 0 && i == 0) c = f16v{}; else c = Qa;
          c = mfma32(a[i + 1], xf[8 * T0 + i + 1], c); Qa = mfma32(a[i], xf[8 * T0 + i], c);
          if constexpr (i + PD < N) { rd(IC<i + PD>{}); rd(IC<i + PD + 1>{}); }
          SB(); }); };
      bar_l();
      STAMP(105);
      { const unsigned xa = row * 256 + ((h ^ (row & 15)) << 4);
        sfor<0, 40>([&](auto s_) { constexpr int s = decltype(s_)::v; xf[s] = ld128(sm, (xa ^ ((s & 7) << 5)) + (s >= 32 ? HB : (s >> 3) * US)); }); }
      bar_l();
      STAMP(106);
      bar_l();
      STAMP(107);
#pragma clang loop unroll(disable)
      for (int gq = 0; gq < 6; ++gq) {
        gq = opqs(gq);
        q_run(IC<0>{}, IC<2>{}, XB);
        seam(gq > 0);
        q_run(IC<2>{}, IC<3>{}, YB);
        { const unsigned a = opq((ha0 ^ (h << 4)) + (gq & 1) * 8192 + 8 * h) ^ (ah << 6);
          sfor<0, 4>([&](auto i_) { constexpr int i = decltype(i_)::v;
            *(LAS u2*)(sm + (a ^ (i << 4))) = u2{pk(Qa[4 * i], Qa[4 * i + 1]), pk(Qa[4 * i + 2], Qa[4 * i + 3])}; }); }
        seam(true);
      }
    }
    {
      f16v acc[10]; bf8 ob[4];
      sfor<0, 10>([&](auto i_) { acc[decltype(i_)::v] = f16v{}; });
      const auto rsP = __builtin_amdgcn_make_buffer_rsrc(part + (size_t)(b * 16 + mem) * 81920 + (qh * 40 + 4 * ah) * 1024, 0, 81920, 0x00020000);
      auto store_t = [&](auto t_) { constexpr int T = decltype(t_)::v; const unsigned pv = lane_id() * 16;
        sfor<0, 4>([&](auto i_) { constexpr int j = decltype(i_)::v >> 1, hf = decltype(i_)::v & 1; const f16v& a = acc[2 * T + j];
          u4 u = {pk(a[8 * hf], a[8 * hf + 1]), pk(a[8 * hf + 2], a[8 * hf + 3]), pk(a[8 * hf + 4], a[8 * hf + 5]), pk(a[8 * hf + 6], a[8 * hf + 7])};
          __builtin_amdgcn_raw_buffer_store_b128(u, rsP, pv, ((4 * T + j) * 2 + hf) * 1024, PART_AUX); }); };
      auto o_run = [&](auto t0_, auto n_, unsigned lb, bool last) { constexpr int T0 = decltype(t0_)::v, N = 8 * decltype(n_)::v;
        const unsigned a0 = opq(oa0 + lb), a1 = a0 ^ 64;
        bf8 a[N];
        auto rd = [&](auto i_) { constexpr int i = decltype(i_)::v; const unsigned ad = ((i & 4) ? a1 : a0) + (i >> 3) * US + (i & 3) * 4096; a[i] = ldtr(sm, ad, ad + 1024); };
        sfor<0, PD>(rd); SB();
        sfor<0, N / 2>([&](auto j_) { constexpr int j = decltype(j_)::v, i = 2 * j, blk = 2 * T0 + (j >> 1), k = (j & 1) * 2;
          f16v c = mfma32(a[i + 1], ob[k + 1], acc[blk]); acc[blk] = mfma32(a[i], ob[k], c);
          if constexpr (i + PD < N) { rd(IC<i + PD>{}); rd(IC<i + PD + 1>{}); }
          SB();
          if constexpr ((j & 3) == 3) { if (last) store_t(IC<T0 + (j >> 2)>{}); } }); };
#pragma clang loop unroll(disable)
      for (int go = 0; go < 6; ++go) {
        go = opqs(go);
        const bool last = go == 5;
        { const unsigned ad = opq(ha0 + (go & 1) * 8192); sfor<0, 4>([&](auto k_) { constexpr int k = decltype(k_)::v; ob[k] = ld128(sm, ad ^ (k << 5)); }); }
        o_run(IC<0>{}, IC<2>{}, XB, last);
        if (!last) seam(true);
        o_run(IC<2>{}, IC<3>{}, YB, last);
        if (last) break;
        seam(go != 4);
        if (go < 3) seam(true);
        if (go == 4) STAMP(100);
      }
    }
    STAMP(102);
    asm volatile("s_waitcnt vmcnt(0)" ::: "memory"); STAMP(103); stampc_(st1, 111);
    STAMP_FLUSH();
  }
}

__global__ void tiny_kernel(u64* st) { st[310000] = (unsigned)__builtin_amdgcn_s_memrealtime(); }
__global__ void spin_kernel(const u64* st, int mode) {
  const unsigned sp0 = (unsigned)__builtin_amdgcn_s_memrealtime();
  unsigned mn = 0xffffffffu, mx = 0, mp = 0, ma = 0, rs = 0xffffffffu, re = 0; const unsigned ref = (unsigned)st[4 * 128];
  for (int i = 0; i < 256; ++i) { const u64* s = st + (i * 8 + 4) * 128; unsigned a = (unsigned)s[0] - ref + 100000, e = (unsigned)s[103] - ref + 100000, p = (unsigned)s[107] - ref + 100000, q = (unsigned)s[100] - ref + 100000;
    mn = a < mn ? a : mn; mx = e > mx ? e : mx; mp = p > mp ? p : mp; ma = q > ma ? q : ma;
    unsigned r0 = (unsigned)st[300000 + 2 * i] - ref + 100000, r1 = (unsigned)st[300001 + 2 * i] - ref + 100000; rs = r0 < rs ? r0 : rs; re = r1 > re ? r1 : re; }
  const u64* s = st + (37 * 8 + 4) * 128; float clk = (float)((unsigned)s[111] - (unsigned)s[110]) / (float)((unsigned)s[103] - (unsigned)s[0]);
  u64 T = mode == 0 ? (u64)(mx - mn) * 20 : mode == 1 ? (u64)(clk * 5000.f) : mode == 2 ? (u64)(mp - mn) * 100 : mode == 3 ? (u64)(ma - mn) * 20 : mode == 4 ? (u64)(rs - mx) * 100 : (u64)(re - rs) * 100;
  if (mode == 6) T = (u64)(mn - ((unsigned)st[310000] - ref + 100000)) * 100;
  if (mode == 7) T = (u64)((sp0 - ref + 100000) - re) * 100;
  if (mode == 8) T = 0;
  if (T > 400000) T = 400000;
  u64 t0 = __builtin_amdgcn_s_memrealtime(); while (__builtin_amdgcn_s_memrealtime() - t0 < T) __builtin_amdgcn_s_sleep(8);
}
__global__ __launch_bounds__(320) void reduce_kernel(const char* __restrict__ part, char* __restrict__ out, u64* st) {
  u64 t0_ = __builtin_amdgcn_s_memrealtime();
  const int b = blockIdx.x >> 4, j = blockIdx.x & 15, t = threadIdx.x;
  const int pi = 5 * j + (t >> 6), r = t & 63, qh = pi / 40, L = (pi % 40) >> 2, o = pi & 3, un = r >> 4, h = (r >> 3) & 1, q8 = r & 7;
  const int nblk = 2 * L + (un >> 1), hf = un & 1;
  const char* src = part + (size_t)b * 16 * 81920 + ((qh * 20 + nblk) * 2 + hf) * 1024 + (32 * h + 8 * o + q8) * 16;
  u4 v[16];
#pragma unroll
  for (int i = 0; i < 16; ++i) v[i] = *(const u4*)(src + (size_t)i * 81920);
  float s[8] = {};
#pragma unroll
  for (int i = 0; i < 16; ++i)
#pragma unroll
    for (int e = 0; e < 4; ++e) { s[2 * e] += __uint_as_float(v[i][e] << 16); s[2 * e + 1] += __uint_as_float(v[i][e] & 0xffff0000u); }
  char* dst = out + (size_t)(b * 64 + 32 * qh + 8 * o + q8) * 1280 + (32 * nblk + 16 * hf + 4 * h) * 2;
  *(u2*)dst = u2{pk(s[0], s[1]), pk(s[2], s[3])};
  *(u2*)(dst + 16) = u2{pk(s[4], s[5]), pk(s[6], s[7])};
  asm volatile("s_waitcnt vmcnt(0)" ::: "memory");
  if (t == 0) { st[300000 + 2 * blockIdx.x] = (unsigned)t0_; st[300001 + 2 * blockIdx.x] = (unsigned)__builtin_amdgcn_s_memrealtime(); }
}

extern "C" void kernel_launch(void* const* d_in, const int* in_sizes, int n_in, void* d_out, int out_size, void* d_ws, size_t ws_size, hipStream_t stream) {
  char* part = (char*)d_ws + (1 << 20);

  fused_kernel<<<256, 512, 0, stream>>>((const char*)d_in[0], (const char*)d_in[1], (const char*)d_in[2], (const char*)d_in[3], (const char*)d_in[4], part
                                        , (u64*)((char*)d_ws + (64 << 20))
  );
  reduce_kernel<<<256, 320, 0, stream>>>(part, (char*)d_out, (u64*)((char*)d_ws + (64 << 20)));
  spin_kernel<<<1, 64, 0, stream>>>((const u64*)((char*)d_ws + (64 << 20)), 8);
}
```

```cpp
#include <hip/hip_runtime.h>
#include <hip/hip_bf16.h>
#include <stdint.h>

typedef short s4 __attribute__((ext_vector_type(4)));
typedef short s8 __attribute__((ext_vector_type(8)));
typedef __bf16 bf8 __attribute__((ext_vector_type(8)));
typedef __bf16 bf2 __attribute__((ext_vector_type(2)));
typedef float f2 __attribute__((ext_vector_type(2)));
typedef float f4 __attribute__((ext_vector_type(4)));
typedef float f16v __attribute__((ext_vector_type(16)));
typedef unsigned u2 __attribute__((ext_vector_type(2)));
typedef unsigned u4 __attribute__((ext_vector_type(4)));
typedef unsigned long long u64;
#define LAS __attribute__((address_space(3)))
#define GAS __attribute__((address_space(1)))
#define DI __device__ __forceinline__

template <int N> struct IC { static constexpr int v = N; };
template <int A, int B, class F> DI void sfor(F&& f) { if constexpr (A < B) { f(IC<A>{}); sfor<A + 1, B>(f); } }

constexpr unsigned US = 16384, KVS = 32768, XB = 65536, YB = 98304, HB = 147456, LDS_BYTES = 163840;
constexpr unsigned PART_OFF = 1 << 20;
constexpr unsigned ITEM = 196608;
constexpr float SC = 0.07216878364870322f * 1.4426950408889634f;

#define PROJ_PRIO 0
#define ATT_PRIO 1
#define DRY 0
#define PART_AUX 16
#define STAMP(i) do {} while (0)
#define STAMPA(i) do {} while (0)
#define STAMP_DECL
#define STAMP_FLUSH() do {} while (0)

#define NOPS
template <class T> DI T rfl(T v) { return v; }
DI u4 mk_srd(const void* p, unsigned bytes) {
  const u64 a = (u64)p;
  u4 r = {(unsigned)__builtin_amdgcn_readfirstlane((unsigned)a), (unsigned)__builtin_amdgcn_readfirstlane((unsigned)(a >> 32) & 0xffffu), bytes, 0x00020000u};
  return r;
}
template <bool NT> DI void dma4(unsigned mb, unsigned v0, unsigned v1, u4 srd, unsigned so, unsigned st) {
  unsigned t;
#define DMA4_BODY(POL) \
  asm volatile(NOPS "s_mov_b32 m0, %1\n\ts_add_u32 %0, %5, %6\n\tbuffer_load_dwordx4 %2, %4, %5 offen " POL "lds\n\t" \
               "s_add_u32 m0, %1, 0x400\n\ts_nop 0\n\tbuffer_load_dwordx4 %3, %4, %0 offen " POL "lds\n\t" \
               "s_add_u32 m0, %1, 0x800\n\ts_add_u32 %0, %0, %6\n\tbuffer_load_dwordx4 %2, %4, %0 offen " POL "lds\n\t" \
               "s_add_u32 m0, %1, 0xc00\n\ts_add_u32 %0, %0, %6\n\tbuffer_load_dwordx4 %3, %4, %0 offen " POL "lds" \
               : "=&s"(t) : "s"(rfl(mb)), "v"(v0), "v"(v1), "s"(rfl(srd)), "s"(rfl(so)), "s"(rfl(st)) : "memory", "scc")
  if constexpr (NT) DMA4_BODY("nt "); else DMA4_BODY("");
#undef DMA4_BODY
}
template <int J> DI void dma1nt(unsigned mb, unsigned voff, u4 srd, unsigned so) {
  unsigned t;
  asm volatile(NOPS "s_add_u32 m0, %1, %2\n\ts_add_u32 %0, %3, %4\n\tbuffer_load_dwordx4 %5, %6, %0 offen nt lds"
               : "=&s"(t) : "s"(rfl(mb)), "i"(J * 1024), "s"(rfl(so)), "i"(J * 3072), "v"(voff), "s"(rfl(srd)) : "memory", "scc");
}
DI void dma1(unsigned m0v, unsigned voff, u4 srd, unsigned so) {
  asm volatile("s_mov_b32 m0, %0\n\ts_nop 0\n\tbuffer_load_dwordx4 %1, %2, %3 offen lds" :: "s"(m0v), "v"(voff), "s"(srd), "s"(so) : "memory");
}
DI void touch4(unsigned m0v, unsigned voff, u4 srd, unsigned so) {
  asm volatile(NOPS "s_mov_b32 m0, %0\n\ts_nop 0\n\tbuffer_load_dword %1, %2, %3 offen lds" :: "s"(rfl(m0v)), "v"(voff), "s"(rfl(srd)), "s"(rfl(so)) : "memory");
}
#define SB() __builtin_amdgcn_sched_barrier(0)
template <int N> DI void bar_v() { SB(); asm volatile("s_waitcnt vmcnt(%0) lgkmcnt(0)\n\ts_barrier" ::"i"(N) : "memory"); SB(); }
DI void bar_l() { SB(); asm volatile("s_waitcnt lgkmcnt(0)\n\ts_barrier" ::: "memory"); SB(); }

DI bf8 ld128(LAS char* sm, unsigned a) { return __builtin_bit_cast(bf8, *(LAS s8*)(sm + a)); }
DI bf8 ldtr(LAS char* sm, unsigned a0, unsigned a1) {
  s4 x = __builtin_amdgcn_ds_read_tr16_b64_v4i16((LAS s4*)(sm + a0)), y = __builtin_amdgcn_ds_read_tr16_b64_v4i16((LAS s4*)(sm + a1));
  return __builtin_bit_cast(bf8, __builtin_shufflevector(x, y, 0, 1, 2, 3, 4, 5, 6, 7));
}
DI unsigned opq(unsigned v) { asm volatile("" : "+v"(v)); return v; }
DI bf8 undef8() { bf8 v; asm volatile("" : "=v"(v)); return v; }
DI unsigned sel(bool c, unsigned a, unsigned b) { return c ? a : b; }
DI int opqs(int v) { v = __builtin_amdgcn_readfirstlane(v); asm volatile("" : "+s"(v)); return v; }
DI int lane_id() { unsigned z = 0; asm volatile("" : "+v"(z)); return __builtin_amdgcn_mbcnt_hi(~0u, __builtin_amdgcn_mbcnt_lo(~0u, z)); }
DI unsigned pk(float a, float b) { f2 x = {a, b}; return __builtin_bit_cast(unsigned, __builtin_convertvector(x, bf2)); }
DI f4 mfma16(bf8 a, bf8 b, f4 c) { return __builtin_amdgcn_mfma_f32_16x16x32_bf16(a, b, c, 0, 0, 0); }
DI f16v mfma32(bf8 a, bf8 b, f16v c) { return __builtin_amdgcn_mfma_f32_32x32x16_bf16(a, b, c, 0, 0, 0); }

constexpr u64 w_table() { u64 t = 0; for (int c = 0; c < 24; ++c) { const int n = c < 12 ? 1 + (c & 1) : (c == 13 || c == 16 || c == 19 || c == 21 || c == 23) ? 1 : (c == 14 || c == 17 || c == 20 || c == 22) ? 2 : 0; t |= (u64)n << (2 * c); } return t; }
#define WHOLE ((1u<<19)|(1u<<21))

__global__ __launch_bounds__(512, 2) void fused_kernel(const char* __restrict__ x, const char* __restrict__ K, const char* __restrict__ V,
                                                       const char* __restrict__ wq, const char* __restrict__ wo, char* ws, char* __restrict__ out
) {
  __shared__ __attribute__((aligned(16))) char smem[LDS_BYTES];
  LAS char* sm = (LAS char*)smem;
  const int wave = __builtin_amdgcn_readfirstlane(threadIdx.x >> 6), w = wave & 3;
  const int xcd = blockIdx.x & 7, idx = blockIdx.x >> 3, b = idx >> 1, hp = idx & 1, h0 = xcd * 4 + hp * 2, mem = xcd * 2 + hp;
  const u4 sK = mk_srd(K + (size_t)(b * 32 + h0) * ITEM, 2 * ITEM), sV = mk_srd(V + (size_t)(b * 32 + h0) * ITEM, 2 * ITEM);
  const u4 sQ = mk_srd(wq + h0 * 384, 640 * 12288 - h0 * 384), sO = mk_srd(wo + h0 * (192 * 1280), 384 * 1280);
  char* const part = ws + PART_OFF;
  GAS u64* const sync = (GAS u64*)(ws + b * 8192);
  const unsigned w4k = w * 4096u, OOB = 0x7ffffff0u;
  auto v_k = [&](int l) { return (unsigned)((32 * w + (l >> 3)) * 384 + (((l & 7) ^ (l >> 4)) << 4)); };

  auto reduce_slice = [&](int i) {
    const int r = lane_id(), pi = 5 * mem + i, qh = pi / 40, L = (pi % 40) >> 2, o = pi & 3, un = r >> 4, h = (r >> 3) & 1, q8 = r & 7;
    const int nblk = 2 * L + (un >> 1), hf = un & 1;
    const auto rs = __builtin_amdgcn_make_buffer_rsrc(part + (size_t)b * 16 * 81920, 0, 16 * 81920, 0x00020000);
    const unsigned vo = ((qh * 20 + nblk) * 2 + hf) * 1024 + (32 * h + 8 * o + q8) * 16;
    u4 v[16];
    sfor<0, 16>([&](auto p_) { constexpr int p = decltype(p_)::v; v[p] = __builtin_amdgcn_raw_buffer_load_b128(rs, vo, p * 81920, 16); });
    float s[8] = {};
    sfor<0, 16>([&](auto p_) { constexpr int p = decltype(p_)::v; sfor<0, 4>([&](auto e_) { constexpr int e = decltype(e_)::v;
      s[2 * e] += __uint_as_float(v[p][e] << 16); s[2 * e + 1] += __uint_as_float(v[p][e] & 0xffff0000u); }); });
    char* dst = out + (size_t)(b * 64 + 32 * qh + 8 * o + q8) * 1280 + (32 * nblk + 16 * hf + 4 * h) * 2;
    *(u2*)dst = u2{pk(s[0], s[1]), pk(s[2], s[3])};
    *(u2*)(dst + 16) = u2{pk(s[4], s[5]), pk(s[6], s[7])}; };

  if (wave < 4) {
    __builtin_amdgcn_s_setprio(ATT_PRIO);
    const int lane = lane_id();
    STAMP_DECL; STAMP(0);
    const int r16 = lane & 15, g = lane >> 4, q4 = (lane & 15) >> 2, p = lane & 3;
    unsigned w_so = 0, w_g = 0, w_v = (32 * w + (lane >> 3)) * 12288 + (((lane & 7) ^ (((lane >> 4) & 1) << 2)) << 4), w_st = 8 * 12288, w_dT = 128 * 12288, w_dG = 128u - 5u * 128 * 12288;
    u4 w_srd = sQ;
    auto w_tile = [&](unsigned lb) { dma4<false>(lb + w4k, w_v, w_v, w_srd, w_so, w_st); w_so += w_dT; };
    auto w_pair = [&]() { w_tile(XB); w_tile(XB + US); };
    auto w_triple = [&]() { w_tile(YB); w_tile(YB + US); w_tile(YB + 2 * US); w_so += w_dG;
      if (++w_g == 6) { const int l = lane_id(); w_so = 0; w_v = (16 * w + (l >> 4)) * 1280 + (((l & 15) ^ ((l >> 4) << 2)) << 4); w_st = 4 * 1280; w_dT = 256; w_dG = 64 * 1280 - 5 * 256; w_srd = sO; } };
    auto w_issue = [&](int c) { const unsigned n = (unsigned)(w_table() >> (2 * c + 2)) & 3; if (n & 1) w_pair(); if (n & 2) w_triple(); };
    const u64 epoch = __hip_atomic_load(sync, __ATOMIC_RELAXED, __HIP_MEMORY_SCOPE_AGENT);
    { const u4 sX = mk_srd(x + b * 81920, 81920);
      const unsigned vX = (16 * w + (lane >> 4)) * 1280 + (((lane & 15) ^ (lane >> 4)) << 4);
      const unsigned t0 = (hp ? HB : 0) + w4k, trow = (b * 40 + w * 10) * 12288;
      touch4(t0, lane < 10 ? trow + lane * 12288 : OOB, sQ, 0);
      { const unsigned vt = lane < 2 ? lane * ITEM : OOB;
        if (w == 0) touch4(t0, vt, sK, 0); else if (w == 1) touch4(t0, vt, sV, 0); else touch4(t0, vt, sO, 0); }
      for (int t = 0; t < 5; ++t) { const unsigned tt = hp ? 4 - t : t, lb = tt == 4 ? HB : tt * US;
        sfor<0, 4>([&](auto j_) { constexpr int j = decltype(j_)::v; dma1(lb + w4k + j * 1024, vX ^ (j << 6), sX, tt * 256 + j * 4 * 1280); }); }
      w_pair();
      STAMP(104);
      bar_v<8>();
      STAMP(105);
      w_triple();
      bar_v<12>();
      STAMP(106);
      touch4(w4k, lane < 20 ? trow + (lane >> 1) * 12288 + (lane & 1) * 128 : OOB, sQ, 128);
      { const unsigned vK = v_k(lane); for (int u = 0; u < 2; ++u) dma4<true>(u * US + w4k, vK, vK ^ 64, sK, u * 49152, 3072); }
      STAMP(107);
      bar_v<9>();
      bar_l();
      w_pair();
      bar_v<12>();
      STAMP(1); }
    const unsigned ka0 = r16 * 128 + ((g ^ ((r16 >> 1) & 7)) << 4);
    const unsigned va0 = (4 * g + q4) * 128 + p * 8 + (((2 * g + (q4 >> 1)) & 3) << 5);
    const unsigned wa0 = (16 * w + r16) * 128 + (((g >> 1) ^ ((r16 >> 1) & 7)) << 4) + (g & 1) * 8 + HB;
    bf8 qc[2], qn[3][2], pf[16]; f4 S[32], O[4], Ls; u2 hA[4]; float m0, mc[2], alpha, linv;
    const u4 ones_u = {0x3f803f80u, 0x3f803f80u, 0x3f803f80u, 0x3f803f80u};
    auto read_q = [&](bf8* q, int hs) { const unsigned a = opq(ka0 + HB + w * 2048 + hs * 8192); q[0] = ld128(sm, a); q[1] = ld128(sm, a ^ 64); };
    auto k_unit = [&](auto kb_, auto un_, auto first_) { constexpr int kb = decltype(kb_)::v, un = decltype(un_)::v, PD = 8; constexpr bool first = decltype(first_)::v;
      const unsigned a0 = opq(ka0) + kb * KVS + un * US, a1 = a0 ^ 64;
      bf8 a[16];
      auto rd = [&](auto i_) { constexpr int i = decltype(i_)::v; a[i] = ld128(sm, ((i & 1) ? a1 : a0) + (i >> 1) * 2048); };
      sfor<0, PD>(rd); SB();
      sfor<0, 8>([&](auto j_) { constexpr int j = decltype(j_)::v, blk = 16 * kb + 8 * un + j;
        f4 c; if constexpr (first) c = f4{0, 0, 0, 0}; else c = S[blk];
        c = mfma16(a[2 * j + 1], qc[1], c); S[blk] = mfma16(a[2 * j], qc[0], c);
        if constexpr (2 * j + PD < 16) { rd(IC<2 * j + PD>{}); rd(IC<2 * j + PD + 1>{}); }
        SB(); }); };
    auto max_half = [&](auto h_) { constexpr int h = decltype(h_)::v;
      float m = fmaxf(S[16 * h][0], S[16 * h][1]), m2 = fmaxf(S[16 * h][2], S[16 * h][3]);
      sfor<1, 16>([&](auto b_) { constexpr int i = 16 * h + decltype(b_)::v; m = fmaxf(fmaxf(m, S[i][0]), S[i][1]); m2 = fmaxf(fmaxf(m2, S[i][2]), S[i][3]); });
      m = fmaxf(m, m2);
      { auto r = __builtin_amdgcn_permlane16_swap(__float_as_uint(m), __float_as_uint(m), false, false); m = fmaxf(__uint_as_float(r[0]), __uint_as_float(r[1])); }
      { auto r = __builtin_amdgcn_permlane32_swap(__float_as_uint(m), __float_as_uint(m), false, false); m = fmaxf(__uint_as_float(r[0]), __uint_as_float(r[1])); }
      if constexpr (h == 0) m0 = m; else { m = fmaxf(m, m0); alpha = __builtin_amdgcn_exp2f((m0 - m) * SC); }
      mc[h] = -m * SC; };
    auto exp_kk = [&](auto kk_) { constexpr int kk = decltype(kk_)::v; float e[8];
      sfor<0, 8>([&](auto j_) { constexpr int j = decltype(j_)::v; e[j] = __builtin_amdgcn_exp2f(fmaf(S[2 * kk + (j >> 2)][j & 3], SC, mc[kk >> 3])); });
      u4 u = {pk(e[0], e[1]), pk(e[2], e[3]), pk(e[4], e[5]), pk(e[6], e[7])}; pf[kk] = __builtin_bit_cast(bf8, u); };
    auto v_unit = [&](auto kb_, auto un_, auto ls_) { constexpr int kb = decltype(kb_)::v, un = decltype(un_)::v, PD = 6; constexpr bool ls = decltype(ls_)::v;
      const unsigned a0 = opq(va0) + kb * KVS + un * US;
      bf8 a[16];
      auto rd = [&](auto i_) { constexpr int i = decltype(i_)::v; const unsigned ad = (a0 ^ ((i & 3) << 5)) + (i >> 2) * 4096; a[i] = ldtr(sm, ad, ad + 2048); };
      sfor<0, PD>(rd);
      if constexpr (kb == 1 && un == 0) { sfor<0, 4>([&](auto d_) { constexpr int db = decltype(d_)::v; O[db] *= alpha; }); if constexpr (ls) Ls *= alpha; }
      SB();
      sfor<0, 8>([&](auto j_) { constexpr int j = decltype(j_)::v, kk = 8 * kb + 4 * un + (j >> 1), d0 = (j & 1) * 2;
        f4 c0, c1; if constexpr (kk == 0) { c0 = f4{0, 0, 0, 0}; c1 = c0; } else { c0 = O[d0]; c1 = O[d0 + 1]; }
        O[d0 + 1] = mfma16(a[2 * j + 1], pf[kk], c1); O[d0] = mfma16(a[2 * j], pf[kk], c0);
        if constexpr (ls && (j & 1)) { f4 cl; if constexpr (kk == 0) cl = f4{0, 0, 0, 0}; else cl = Ls; Ls = mfma16(__builtin_bit_cast(bf8, ones_u), pf[kk], cl); }
        if constexpr (2 * j + PD < 16) { rd(IC<2 * j + PD>{}); rd(IC<2 * j + PD + 1>{}); }
        SB(); }); };
    auto finish_o = [&]() { sfor<0, 4>([&](auto d_) { constexpr int db = decltype(d_)::v;
        hA[db] = u2{pk(O[db][0] * linv, O[db][1] * linv), pk(O[db][2] * linv, O[db][3] * linv)}; }); };
    auto write_o = [&](int hs) { const unsigned a = opq(wa0 + hs * 8192);
      sfor<0, 4>([&](auto d_) { constexpr int db = decltype(d_)::v; *(LAS u2*)(sm + (a ^ (db << 5))) = hA[db]; }); };
    sfor<0, 3>([&](auto i_) { qn[decltype(i_)::v][0] = bf8{}; qn[decltype(i_)::v][1] = bf8{}; });

    auto bar_a = [&](int c, bool second) { if ((((unsigned)WHOLE >> c) & 1) == (unsigned)second) { STAMP(6 + 4 * c); bar_v<0>(); } };
    auto w_and_c = [&](int c) { STAMP(5 + 4 * c); w_issue(c); STAMP(7 + 4 * c); bar_l(); };
#pragma clang loop unroll(disable)
    for (int it = 0; it < 2; ++it) {
      it = opqs(it);
      const int c0 = 12 * it;
      sfor<0, 6>([&](auto r_) { constexpr int r = decltype(r_)::v, ch = r >> 1, kb = r & 1;
        if constexpr (kb == 0) { bf8 n[2] = {qn[ch][0], qn[ch][1]}; if (it == 0) read_q(n, ch & 1); qc[0] = n[0]; qc[1] = n[1]; }
        k_unit(IC<kb>{}, IC<0>{}, IC<ch == 0>{});
        bar_a(c0 + r, false);
        k_unit(IC<kb>{}, IC<1>{}, IC<ch == 0>{});
        bar_a(c0 + r, true);
        if constexpr (r == 4) { max_half(IC<0>{}); sfor<0, 1>(exp_kk); }
        if constexpr (r == 5) sfor<1, 5>(exp_kk);
        w_and_c(c0 + r); });
      if (it == 0) read_q(qn[0], 1);
      v_unit(IC<0>{}, IC<0>{}, IC<1>{}); sfor<5, 8>(exp_kk);
      bar_a(c0 + 6, false);
      v_unit(IC<0>{}, IC<1>{}, IC<1>{});
      bar_a(c0 + 6, true);
      max_half(IC<1>{}); sfor<8, 12>(exp_kk);
      w_and_c(c0 + 6);
      v_unit(IC<1>{}, IC<0>{}, IC<1>{}); sfor<12, 16>(exp_kk);
      bar_a(c0 + 7, false);
      v_unit(IC<1>{}, IC<1>{}, IC<1>{}); linv = 1.0f / Ls[0]; finish_o();
      if (it == 1) write_o(1);
      bar_a(c0 + 7, true);
      w_and_c(c0 + 7);
#pragma clang loop unroll(disable)
      for (int ch = 1; ch < 3; ++ch) {
        ch = opqs(ch);
        const int c = c0 + 6 + 2 * ch;
        if (it == 0) { bf8 n[2]; read_q(n, (ch + 1) & 1); qn[1][0] = qn[2][0]; qn[1][1] = qn[2][1]; qn[2][0] = n[0]; qn[2][1] = n[1]; }
        v_unit(IC<0>{}, IC<0>{}, IC<0>{});
        bar_a(c, false);
        v_unit(IC<0>{}, IC<1>{}, IC<0>{});
        bar_a(c, true);
        if (it == 0) write_o((ch - 1) & 1);
        w_and_c(c);
        if (c == 22) w_triple();
        v_unit(IC<1>{}, IC<0>{}, IC<0>{});
        if (c == 22) bar_l(); else bar_a(c + 1, false);
        v_unit(IC<1>{}, IC<1>{}, IC<0>{}); finish_o();
        if (it == 1 || ch == 2) write_o((it + ch) & 1);
        if (c == 22) break;
        bar_a(c + 1, true);
        w_and_c(c + 1);
      }
    }
    bar_v<12>();
    bar_v<0>();
    bar_l();
    if (w == 0) {
      const int l = lane_id(); const u64 tag = epoch + 1;
      if (l == 0) __hip_atomic_store(sync + 32 * (1 + mem), tag, __ATOMIC_RELAXED, __HIP_MEMORY_SCOPE_AGENT);
      for (unsigned spins = 0; spins < (1u << 20); ++spins) {
        u64 f = tag; if (l < 16) f = __hip_atomic_load(sync + 32 * (1 + l), __ATOMIC_RELAXED, __HIP_MEMORY_SCOPE_AGENT);
        if (__all(f == tag)) break;
        __builtin_amdgcn_s_sleep(1); }
      if (l == 0 && mem == 0) __hip_atomic_store(sync, tag, __ATOMIC_RELAXED, __HIP_MEMORY_SCOPE_AGENT);
      __builtin_amdgcn_fence(__ATOMIC_ACQUIRE, "wavefront");
    }
    bar_l();
    reduce_slice(w);
    STAMP_FLUSH();
  } else {
    __builtin_amdgcn_s_setprio(PROJ_PRIO);
    const int lane = lane_id();
    STAMP_DECL; STAMP(0);
    const int qh = w & 1, ah = w >> 1, h = lane >> 5, b16 = (lane >> 4) & 1, q4 = (lane & 15) >> 2, p = lane & 3;
    const unsigned row = 32 * qh + (lane & 31), f8 = (row >> 1) & 7;
    const unsigned qa0 = (8 * h + q4) * 128 + ((ah ^ (q4 >> 1)) << 6) + 32 * b16 + 8 * p;
    const unsigned oa0 = (8 * h + q4) * 256 + (((2 * ah) ^ q4) << 6) + 32 * b16 + 8 * p;
    const unsigned ha0 = row * 128 + ((h ^ f8) << 4) + HB;
    constexpr int PD = 6;
    const unsigned vK = v_k(lane), vV = (32 * w + (lane >> 3)) * 384 + (((lane & 7) ^ (((lane >> 4) & 3) << 1)) << 4);
    unsigned k_so = 2 * 49152, k_n = 1, k_vE = vK, k_vO = vK ^ 64; u4 k_srd = sK;
    auto kv_tile = [&]() { const unsigned lb = (k_n & 1) * KVS + w4k;
      dma4<true>(lb, k_vE, k_vO, k_srd, k_so, 3072); dma4<true>(lb + US, k_vE, k_vO, k_srd, k_so + 49152, 3072);
      ++k_n; k_so += (k_n & 1) ? 2 * 49152u : 128u - 2 * 49152u;
      if (k_n == 6 || k_n == 12 || k_n == 18) { const bool isV = k_n != 12;
        k_so = k_n == 6 ? 0 : ITEM; k_srd[0] = sel(isV, sV[0], sK[0]); k_srd[1] = sel(isV, sV[1], sK[1]); k_vE = sel(isV, vV, vK); k_vO = sel(isV, vV, vK ^ 64); } };
    auto seam = [&](bool kv) { STAMP(k_n >= 2 ? 4 * k_n - 3 : 2);
      if (((unsigned)WHOLE >> (k_n - 1)) & 1) bar_v<0>(); else bar_v<4>();
      STAMP(k_n >= 2 ? 4 * k_n - 2 : 1);
      if (kv) {
        if (k_n == 2 || k_n == 7 || k_n == 15) { const unsigned l = lane_id(), tl = b * 120 + w * 30 + l; const bool q = k_n == 2;
          const u4 st = {sel(q, sQ[0], sO[0]), sel(q, sQ[1], sO[1]), sel(q, sQ[2], sO[2]), sQ[3]};
          touch4((k_n & 1) * KVS + w4k, l < 30 ? sel(q, (tl / 3) * 12288 + (tl % 3) * 128, tl * 128) : OOB, st, q ? 384 : k_n == 7 ? 0 : 245760); }
        kv_tile(); STAMP(k_n >= 3 ? 4 * k_n - 5 : 2); bar_v<8>(); } else bar_v<0>();
      STAMPA(k_n >= 2 ? 4 * k_n - 4 : 2); };

#pragma clang loop unroll(disable)
    for (int pass = DRY ? 0 : 1; pass < 2; ++pass) {
    pass = opqs(pass);
    const bool dry = pass == 0;
    {
      bf8 xf[40]; f16v Qa = {};
      sfor<0, 40>([&](auto s_) { xf[decltype(s_)::v] = undef8(); });
      auto q_run = [&](auto t0_, auto n_, unsigned lb) { constexpr int T0 = decltype(t0_)::v, N = 8 * decltype(n_)::v;
        const unsigned a0 = opq(qa0 + lb);
        bf8 a[N];
        auto rd = [&](auto i_) { constexpr int i = decltype(i_)::v; a[i] = ldtr(sm, a0 + i * 2048, a0 + i * 2048 + 512); };
        sfor<0, PD>(rd); SB();
        sfor<0, N / 2>([&](auto j_) { constexpr int i = 2 * decltype(j_)::v;
          f16v c; if constexpr (T0 == 0 && i == 0) c = f16v{}; else c = Qa;
          c = mfma32(a[i + 1], xf[8 * T0 + i + 1], c); Qa = mfma32(a[i], xf[8 * T0 + i], c);
          if constexpr (i + PD < N) { rd(IC<i + PD>{}); rd(IC<i + PD + 1>{}); }
          SB(); }); };
      if (!dry) { bar_l();
        STAMP(105);
        const unsigned xa = row * 256 + ((h ^ (row & 15)) << 4);
        sfor<0, 40>([&](auto s_) { constexpr int s = decltype(s_)::v; xf[s] = ld128(sm, (xa ^ ((s & 7) << 5)) + (s >= 32 ? HB : (s >> 3) * US)); });
        bar_l();
        STAMP(106); }
#pragma clang loop unroll(disable)
      for (int gq = 0; gq < 6; ++gq) {
        gq = opqs(gq);
        q_run(IC<0>{}, IC<2>{}, XB);
        if (!dry) seam(gq > 0);
        q_run(IC<2>{}, IC<3>{}, YB);
        if (dry) break;
        { const unsigned a = opq((ha0 ^ (h << 4)) + (gq & 1) * 8192 + 8 * h) ^ (ah << 6);
          sfor<0, 4>([&](auto i_) { constexpr int i = decltype(i_)::v;
            *(LAS u2*)(sm + (a ^ (i << 4))) = u2{pk(Qa[4 * i], Qa[4 * i + 1]), pk(Qa[4 * i + 2], Qa[4 * i + 3])}; }); }
        seam(true);
      }
    }
    {
      f16v acc[10]; bf8 ob[4];
      sfor<0, 10>([&](auto i_) { acc[decltype(i_)::v] = f16v{}; });
      const auto rsP = __builtin_amdgcn_make_buffer_rsrc(part + (size_t)(b * 16 + mem) * 81920 + (qh * 40 + 4 * ah) * 1024, 0, dry ? 0 : 81920, 0x00020000);
      auto store_t = [&](auto t_) { constexpr int T = decltype(t_)::v; const unsigned pv = lane_id() * 16;
        sfor<0, 4>([&](auto i_) { constexpr int j = decltype(i_)::v >> 1, hf = decltype(i_)::v & 1; const f16v& a = acc[2 * T + j];
          u4 u = {pk(a[8 * hf], a[8 * hf + 1]), pk(a[8 * hf + 2], a[8 * hf + 3]), pk(a[8 * hf + 4], a[8 * hf + 5]), pk(a[8 * hf + 6], a[8 * hf + 7])};
          __builtin_amdgcn_raw_buffer_store_b128(u, rsP, pv, ((4 * T + j) * 2 + hf) * 1024, PART_AUX); }); };
      auto o_run = [&](auto t0_, auto n_, unsigned lb, bool last) { constexpr int T0 = decltype(t0_)::v, N = 8 * decltype(n_)::v;
        const unsigned a0 = opq(oa0 + lb), a1 = a0 ^ 64;
        bf8 a[N];
        auto rd = [&](auto i_) { constexpr int i = decltype(i_)::v; const unsigned ad = ((i & 4) ? a1 : a0) + (i >> 3) * US + (i & 3) * 4096; a[i] = ldtr(sm, ad, ad + 1024); };
        sfor<0, PD>(rd); SB();
        sfor<0, N / 2>([&](auto j_) { constexpr int j = decltype(j_)::v, i = 2 * j, blk = 2 * T0 + (j >> 1), k = (j & 1) * 2;
          f16v c = mfma32(a[i + 1], ob[k + 1], acc[blk]); acc[blk] = mfma32(a[i], ob[k], c);
          if constexpr (i + PD < N) { rd(IC<i + PD>{}); rd(IC<i + PD + 1>{}); }
          SB();
          if constexpr ((j & 3) == 3) { if (last) store_t(IC<T0 + (j >> 2)>{}); } }); };
#pragma clang loop unroll(disable)
      for (int go = dry ? 5 : 0; go < 6; ++go) {
        go = opqs(go);
        const bool last = go == 5;
        { const unsigned ad = opq(ha0 + (go & 1) * 8192); sfor<0, 4>([&](auto k_) { constexpr int k = decltype(k_)::v; ob[k] = ld128(sm, ad ^ (k << 5)); }); }
        o_run(IC<0>{}, IC<2>{}, XB, last);
        if (!last) seam(true); else if (!dry) bar_l();
        o_run(IC<2>{}, IC<3>{}, YB, last);
        if (last) break;
        seam(go != 4);
        if (go < 3) seam(true);
        if (go == 4) { STAMP(100); bar_l(); STAMP(101); }
      }
    }
    }
    STAMP(102);
    bar_v<0>();
    STAMP(103);
    bar_l();
    STAMP(124);
    if (w == 0) reduce_slice(4);
    STAMP_FLUSH();
  }
}

extern "C" void kernel_launch(void* const* d_in, const int* in_sizes, int n_in, void* d_out, int out_size, void* d_ws, size_t ws_size, hipStream_t stream) {
  fused_kernel<<<256, 512, 0, stream>>>((const char*)d_in[0], (const char*)d_in[1], (const char*)d_in[2], (const char*)d_in[3], (const char*)d_in[4], (char*)d_ws, (char*)d_out
  );
}
```

```cpp
#include <hip/hip_runtime.h>
#include <hip/hip_bf16.h>
#include <stdint.h>
#define STAMPS
#define LITE

typedef short s4 __attribute__((ext_vector_type(4)));
typedef short s8 __attribute__((ext_vector_type(8)));
typedef __bf16 bf8 __attribute__((ext_vector_type(8)));
typedef __bf16 bf2 __attribute__((ext_vector_type(2)));
typedef float f2 __attribute__((ext_vector_type(2)));
typedef float f4 __attribute__((ext_vector_type(4)));
typedef float f16v __attribute__((ext_vector_type(16)));
typedef unsigned u2 __attribute__((ext_vector_type(2)));
typedef unsigned u4 __attribute__((ext_vector_type(4)));
typedef unsigned long long u64;
#define LAS __attribute__((address_space(3)))
#define GAS __attribute__((address_space(1)))
#define DI __device__ __forceinline__

template <int N> struct IC { static constexpr int v = N; };
template <int A, int B, class F> DI void sfor(F&& f) { if constexpr (A < B) { f(IC<A>{}); sfor<A + 1, B>(f); } }

constexpr unsigned US = 16384, KVS = 32768, XB = 65536, YB = 98304, HB = 147456, LDS_BYTES = 163840;
constexpr unsigned PART_OFF = 1 << 20;
constexpr unsigned ITEM = 196608;
constexpr float SC = 0.07216878364870322f * 1.4426950408889634f;

#define PROJ_PRIO 0
#define ATT_PRIO 1
#define DRY 0
#define PART_AUX 16
DI void stamp_(unsigned& s0, unsigned& s1, int i) { u64 t; __builtin_amdgcn_sched_barrier(0);
  asm volatile("s_memrealtime %0\n\ts_waitcnt lgkmcnt(0)" : "=s"(t) :: "memory");
  asm volatile("s_mov_b32 m0, %3\n\ts_nop 3\n\tv_writelane_b32 %0, %2, m0\n\ts_mov_b32 m0, %4\n\ts_nop 3\n\tv_writelane_b32 %1, %2, m0" : "+v"(s0), "+v"(s1) : "s"((unsigned)t), "s"(__builtin_amdgcn_readfirstlane(i < 64 ? i : 2)), "s"(__builtin_amdgcn_readfirstlane(i >= 64 ? i - 64 : 63)));
  __builtin_amdgcn_sched_barrier(0); }
#define STAMP(i) do { if (__builtin_constant_p(i) && ((i) < 2 || (i) >= 100)) stamp_(st0, st1, (i)); } while (0)
#define STAMPA(i) stamp_(st0, st1, (i))
#define STAMP_DECL unsigned st0 = 0, st1 = 0
#define STAMP_FLUSH() do { const int l_ = lane_id(); stamps[(blockIdx.x * 8 + wave) * 128 + l_] = st0; stamps[(blockIdx.x * 8 + wave) * 128 + 64 + l_] = st1; } while (0)

#define NOPS "s_nop 4\n\t"
DI unsigned rfl(unsigned v) { return __builtin_amdgcn_readfirstlane(v); }
DI u4 rfl(u4 v) { return u4{rfl(v[0]), rfl(v[1]), rfl(v[2]), rfl(v[3])}; }
DI u4 mk_srd(const void* p, unsigned bytes) {
  const u64 a = (u64)p;
  u4 r = {(unsigned)__builtin_amdgcn_readfirstlane((unsigned)a), (unsigned)__builtin_amdgcn_readfirstlane((unsigned)(a >> 32) & 0xffffu), bytes, 0x00020000u};
  return r;
}
template <bool NT> DI void dma4(unsigned mb, unsigned v0, unsigned v1, u4 srd, unsigned so, unsigned st) {
  unsigned t;
#define DMA4_BODY(POL) \
  asm volatile(NOPS "s_mov_b32 m0, %1\n\ts_add_u32 %0, %5, %6\n\tbuffer_load_dwordx4 %2, %4, %5 offen " POL "lds\n\t" \
               "s_add_u32 m0, %1, 0x400\n\ts_nop 0\n\tbuffer_load_dwordx4 %3, %4, %0 offen " POL "lds\n\t" \
               "s_add_u32 m0, %1, 0x800\n\ts_add_u32 %0, %0, %6\n\tbuffer_load_dwordx4 %2, %4, %0 offen " POL "lds\n\t" \
               "s_add_u32 m0, %1, 0xc00\n\ts_add_u32 %0, %0, %6\n\tbuffer_load_dwordx4 %3, %4, %0 offen " POL "lds" \
               : "=&s"(t) : "s"(rfl(mb)), "v"(v0), "v"(v1), "s"(rfl(srd)), "s"(rfl(so)), "s"(rfl(st)) : "memory", "scc")
  if constexpr (NT) DMA4_BODY("nt "); else DMA4_BODY("");
#undef DMA4_BODY
}
template <int J> DI void dma1nt(unsigned mb, unsigned voff, u4 srd, unsigned so) {
  unsigned t;
  asm volatile(NOPS "s_add_u32 m0, %1, %2\n\ts_add_u32 %0, %3, %4\n\tbuffer_load_dwordx4 %5, %6, %0 offen nt lds"
               : "=&s"(t) : "s"(rfl(mb)), "i"(J * 1024), "s"(rfl(so)), "i"(J * 3072), "v"(voff), "s"(rfl(srd)) : "memory", "scc");
}
DI void dma1(unsigned m0v, unsigned voff, u4 srd, unsigned so) {
  asm volatile("s_mov_b32 m0, %0\n\ts_nop 0\n\tbuffer_load_dwordx4 %1, %2, %3 offen lds" :: "s"(m0v), "v"(voff), "s"(srd), "s"(so) : "memory");
}
DI void touch4(unsigned m0v, unsigned voff, u4 srd, unsigned so) {
  asm volatile(NOPS "s_mov_b32 m0, %0\n\ts_nop 0\n\tbuffer_load_dword %1, %2, %3 offen lds" :: "s"(rfl(m0v)), "v"(voff), "s"(rfl(srd)), "s"(rfl(so)) : "memory");
}
#define SB() __builtin_amdgcn_sched_barrier(0)
template <int N> DI void bar_v() { SB(); asm volatile("s_waitcnt vmcnt(%0) lgkmcnt(0)\n\ts_barrier" ::"i"(N) : "memory"); SB(); }
DI void bar_l() { SB(); asm volatile("s_waitcnt lgkmcnt(0)\n\ts_barrier" ::: "memory"); SB(); }

DI bf8 ld128(LAS char* sm, unsigned a) { return __builtin_bit_cast(bf8, *(LAS s8*)(sm + a)); }
DI bf8 ldtr(LAS char* sm, unsigned a0, unsigned a1) {
  s4 x = __builtin_amdgcn_ds_read_tr16_b64_v4i16((LAS s4*)(sm + a0)), y = __builtin_amdgcn_ds_read_tr16_b64_v4i16((LAS s4*)(sm + a1));
  return __builtin_bit_cast(bf8, __builtin_shufflevector(x, y, 0, 1, 2, 3, 4, 5, 6, 7));
}
DI unsigned opq(unsigned v) { asm volatile("" : "+v"(v)); return v; }
DI bf8 undef8() { bf8 v; asm volatile("" : "=v"(v)); return v; }
DI unsigned sel(bool c, unsigned a, unsigned b) { return c ? a : b; }
DI int opqs(int v) { v = __builtin_amdgcn_readfirstlane(v); asm volatile("" : "+s"(v)); return v; }
DI int lane_id() { unsigned z = 0; asm volatile("" : "+v"(z)); return __builtin_amdgcn_mbcnt_hi(~0u, __builtin_amdgcn_mbcnt_lo(~0u, z)); }
DI unsigned pk(float a, float b) { f2 x = {a, b}; return __builtin_bit_cast(unsigned, __builtin_convertvector(x, bf2)); }
DI f4 mfma16(bf8 a, bf8 b, f4 c) { return __builtin_amdgcn_mfma_f32_16x16x32_bf16(a, b, c, 0, 0, 0); }
DI f16v mfma32(bf8 a, bf8 b, f16v c) { return __builtin_amdgcn_mfma_f32_32x32x16_bf16(a, b, c, 0, 0, 0); }

constexpr u64 w_table() { u64 t = 0; for (int c = 0; c < 24; ++c) { const int n = c < 12 ? 1 + (c & 1) : (c == 13 || c == 16 || c == 19 || c == 21 || c == 23) ? 1 : (c == 14 || c == 17 || c == 20 || c == 22) ? 2 : 0; t |= (u64)n << (2 * c); } return t; }
#define WHOLE ((1u<<19)|(1u<<21))

__global__ __launch_bounds__(512, 2) void fused_kernel(const char* __restrict__ x, const char* __restrict__ K, const char* __restrict__ V,
                                                       const char* __restrict__ wq, const char* __restrict__ wo, char* ws, char* __restrict__ out
                                                       , u64* __restrict__ stamps
) {
  u64 t_first; asm volatile("s_memrealtime %0" : "=s"(t_first)); __builtin_amdgcn_sched_barrier(0);
  __shared__ __attribute__((aligned(16))) char smem[LDS_BYTES];
  LAS char* sm = (LAS char*)smem;
  const int wave = __builtin_amdgcn_readfirstlane(threadIdx.x >> 6), w = wave & 3;
  const int xcd = blockIdx.x & 7, idx = blockIdx.x >> 3, b = idx >> 1, hp = idx & 1, h0 = xcd * 4 + hp * 2, mem = xcd * 2 + hp;
  const u4 sK = mk_srd(K + (size_t)(b * 32 + h0) * ITEM, 2 * ITEM), sV = mk_srd(V + (size_t)(b * 32 + h0) * ITEM, 2 * ITEM);
  const u4 sQ = mk_srd(wq + h0 * 384, 640 * 12288 - h0 * 384), sO = mk_srd(wo + h0 * (192 * 1280), 384 * 1280);
  char* const part = ws + PART_OFF;
  GAS u64* const sync = (GAS u64*)(ws + b * 8192);
  const unsigned w4k = w * 4096u, OOB = 0x7ffffff0u;
  auto v_k = [&](int l) { return (unsigned)((32 * w + (l >> 3)) * 384 + (((l & 7) ^ (l >> 4)) << 4)); };

  auto reduce_slice = [&](int i) {
    const int r = lane_id(), pi = 5 * mem + i, qh = pi / 40, L = (pi % 40) >> 2, o = pi & 3, un = r >> 4, h = (r >> 3) & 1, q8 = r & 7;
    const int nblk = 2 * L + (un >> 1), hf = un & 1;
    const auto rs = __builtin_amdgcn_make_buffer_rsrc(part + (size_t)b * 16 * 81920, 0, 16 * 81920, 0x00020000);
    const unsigned vo = ((qh * 20 + nblk) * 2 + hf) * 1024 + (32 * h + 8 * o + q8) * 16;
    u4 v[16];
    sfor<0, 16>([&](auto p_) { constexpr int p = decltype(p_)::v; v[p] = __builtin_amdgcn_raw_buffer_load_b128(rs, vo, p * 81920, 16); });
    float s[8] = {};
    sfor<0, 16>([&](auto p_) { constexpr int p = decltype(p_)::v; sfor<0, 4>([&](auto e_) { constexpr int e = decltype(e_)::v;
      s[2 * e] += __uint_as_float(v[p][e] << 16); s[2 * e + 1] += __uint_as_float(v[p][e] & 0xffff0000u); }); });
    char* dst = out + (size_t)(b * 64 + 32 * qh + 8 * o + q8) * 1280 + (32 * nblk + 16 * hf + 4 * h) * 2;
    *(u2*)dst = u2{pk(s[0], s[1]), pk(s[2], s[3])};
    *(u2*)(dst + 16) = u2{pk(s[4], s[5]), pk(s[6], s[7])}; };

  if (wave < 4) {
    __builtin_amdgcn_s_setprio(ATT_PRIO);
    const int lane = lane_id();
    STAMP_DECL; STAMP(0);
    const int r16 = lane & 15, g = lane >> 4, q4 = (lane & 15) >> 2, p = lane & 3;
    unsigned w_so = 0, w_g = 0, w_v = (32 * w + (lane >> 3)) * 12288 + (((lane & 7) ^ (((lane >> 4) & 1) << 2)) << 4), w_st = 8 * 12288, w_dT = 128 * 12288, w_dG = 128u - 5u * 128 * 12288;
    u4 w_srd = sQ;
    auto w_tile = [&](unsigned lb) { dma4<false>(lb + w4k, w_v, w_v, w_srd, w_so, w_st); w_so += w_dT; };
    auto w_pair = [&]() { w_tile(XB); w_tile(XB + US); };
    auto w_triple = [&]() { w_tile(YB); w_tile(YB + US); w_tile(YB + 2 * US); w_so += w_dG;
      if (++w_g == 6) { const int l = lane_id(); w_so = 0; w_v = (16 * w + (l >> 4)) * 1280 + (((l & 15) ^ ((l >> 4) << 2)) << 4); w_st = 4 * 1280; w_dT = 256; w_dG = 64 * 1280 - 5 * 256; w_srd = sO; } };
    auto w_issue = [&](int c) { const unsigned n = (unsigned)(w_table() >> (2 * c + 2)) & 3; if (n & 1) w_pair(); if (n & 2) w_triple(); };
    const u64 epoch = __hip_atomic_load(sync, __ATOMIC_RELAXED, __HIP_MEMORY_SCOPE_AGENT);
    { const u4 sX = mk_srd(x + b * 81920, 81920);
      const unsigned vX = (16 * w + (lane >> 4)) * 1280 + (((lane & 15) ^ (lane >> 4)) << 4);
      const unsigned t0 = (hp ? HB : 0) + w4k, trow = (b * 40 + w * 10) * 12288;
      touch4(t0, lane < 10 ? trow + lane * 12288 : OOB, sQ, 0);
      { const unsigned vt = lane < 2 ? lane * ITEM : OOB;
        if (w == 0) touch4(t0, vt, sK, 0); else if (w == 1) touch4(t0, vt, sV, 0); else touch4(t0, vt, sO, 0); }
      for (int t = 0; t < 5; ++t) { const unsigned tt = hp ? 4 - t : t, lb = tt == 4 ? HB : tt * US;
        sfor<0, 4>([&](auto j_) { constexpr int j = decltype(j_)::v; dma1(lb + w4k + j * 1024, vX ^ (j << 6), sX, tt * 256 + j * 4 * 1280); }); }
      w_pair();
      STAMP(104);
      bar_v<8>();
      STAMP(105);
      w_triple();
      bar_v<12>();
      STAMP(106);
      touch4(w4k, lane < 20 ? trow + (lane >> 1) * 12288 + (lane & 1) * 128 : OOB, sQ, 128);
      { const unsigned vK = v_k(lane); for (int u = 0; u < 2; ++u) dma4<true>(u * US + w4k, vK, vK ^ 64, sK, u * 49152, 3072); }
      STAMP(107);
      bar_v<9>();
      bar_l();
      w_pair();
      bar_v<12>();
      STAMP(1); }
    const unsigned ka0 = r16 * 128 + ((g ^ ((r16 >> 1) & 7)) << 4);
    const unsigned va0 = (4 * g + q4) * 128 + p * 8 + (((2 * g + (q4 >> 1)) & 3) << 5);
    const unsigned wa0 = (16 * w + r16) * 128 + (((g >> 1) ^ ((r16 >> 1) & 7)) << 4) + (g & 1) * 8 + HB;
    bf8 qc[2], qn[3][2], pf[16]; f4 S[32], O[4], Ls; u2 hA[4]; float m0, mc[2], alpha, linv;
    const u4 ones_u = {0x3f803f80u, 0x3f803f80u, 0x3f803f80u, 0x3f803f80u};
    auto read_q = [&](bf8* q, int hs) { const unsigned a = opq(ka0 + HB + w * 2048 + hs * 8192); q[0] = ld128(sm, a); q[1] = ld128(sm, a ^ 64); };
    auto k_unit = [&](auto kb_, auto un_, auto first_) { constexpr int kb = decltype(kb_)::v, un = decltype(un_)::v, PD = 8; constexpr bool first = decltype(first_)::v;
      const unsigned a0 = opq(ka0) + kb * KVS + un * US, a1 = a0 ^ 64;
      bf8 a[16];
      auto rd = [&](auto i_) { constexpr int i = decltype(i_)::v; a[i] = ld128(sm, ((i & 1) ? a1 : a0) + (i >> 1) * 2048); };
      sfor<0, PD>(rd); SB();
      sfor<0, 8>([&](auto j_) { constexpr int j = decltype(j_)::v, blk = 16 * kb + 8 * un + j;
        f4 c; if constexpr (first) c = f4{0, 0, 0, 0}; else c = S[blk];
        c = mfma16(a[2 * j + 1], qc[1], c); S[blk] = mfma16(a[2 * j], qc[0], c);
        if constexpr (2 * j + PD < 16) { rd(IC<2 * j + PD>{}); rd(IC<2 * j + PD + 1>{}); }
        SB(); }); };
    auto max_half = [&](auto h_) { constexpr int h = decltype(h_)::v;
      float m = fmaxf(S[16 * h][0], S[16 * h][1]), m2 = fmaxf(S[16 * h][2], S[16 * h][3]);
      sfor<1, 16>([&](auto b_) { constexpr int i = 16 * h + decltype(b_)::v; m = fmaxf(fmaxf(m, S[i][0]), S[i][1]); m2 = fmaxf(fmaxf(m2, S[i][2]), S[i][3]); });
      m = fmaxf(m, m2);
      { auto r = __builtin_amdgcn_permlane16_swap(__float_as_uint(m), __float_as_uint(m), false, false); m = fmaxf(__uint_as_float(r[0]), __uint_as_float(r[1])); }
      { auto r = __builtin_amdgcn_permlane32_swap(__float_as_uint(m), __float_as_uint(m), false, false); m = fmaxf(__uint_as_float(r[0]), __uint_as_float(r[1])); }
      if constexpr (h == 0) m0 = m; else { m = fmaxf(m, m0); alpha = __builtin_amdgcn_exp2f((m0 - m) * SC); }
      mc[h] = -m * SC; };
    auto exp_kk = [&](auto kk_) { constexpr int kk = decltype(kk_)::v; float e[8];
      sfor<0, 8>([&](auto j_) { constexpr int j = decltype(j_)::v; e[j] = __builtin_amdgcn_exp2f(fmaf(S[2 * kk + (j >> 2)][j & 3], SC, mc[kk >> 3])); });
      u4 u = {pk(e[0], e[1]), pk(e[2], e[3]), pk(e[4], e[5]), pk(e[6], e[7])}; pf[kk] = __builtin_bit_cast(bf8, u); };
    auto v_unit = [&](auto kb_, auto un_, auto ls_) { constexpr int kb = decltype(kb_)::v, un = decltype(un_)::v, PD = 6; constexpr bool ls = decltype(ls_)::v;
      const unsigned a0 = opq(va0) + kb * KVS + un * US;
      bf8 a[16];
      auto rd = [&](auto i_) { constexpr int i = decltype(i_)::v; const unsigned ad = (a0 ^ ((i & 3) << 5)) + (i >> 2) * 4096; a[i] = ldtr(sm, ad, ad + 2048); };
      sfor<0, PD>(rd);
      if constexpr (kb == 1 && un == 0) { sfor<0, 4>([&](auto d_) { constexpr int db = decltype(d_)::v; O[db] *= alpha; }); if constexpr (ls) Ls *= alpha; }
      SB();
      sfor<0, 8>([&](auto j_) { constexpr int j = decltype(j_)::v, kk = 8 * kb + 4 * un + (j >> 1), d0 = (j & 1) * 2;
        f4 c0, c1; if constexpr (kk == 0) { c0 = f4{0, 0, 0, 0}; c1 = c0; } else { c0 = O[d0]; c1 = O[d0 + 1]; }
        O[d0 + 1] = mfma16(a[2 * j + 1], pf[kk], c1); O[d0] = mfma16(a[2 * j], pf[kk], c0);
        if constexpr (ls && (j & 1)) { f4 cl; if constexpr (kk == 0) cl = f4{0, 0, 0, 0}; else cl = Ls; Ls = mfma16(__builtin_bit_cast(bf8, ones_u), pf[kk], cl); }
        if constexpr (2 * j + PD < 16) { rd(IC<2 * j + PD>{}); rd(IC<2 * j + PD + 1>{}); }
        SB(); }); };
    auto finish_o = [&]() { sfor<0, 4>([&](auto d_) { constexpr int db = decltype(d_)::v;
        hA[db] = u2{pk(O[db][0] * linv, O[db][1] * linv), pk(O[db][2] * linv, O[db][3] * linv)}; }); };
    auto write_o = [&](int hs) { const unsigned a = opq(wa0 + hs * 8192);
      sfor<0, 4>([&](auto d_) { constexpr int db = decltype(d_)::v; *(LAS u2*)(sm + (a ^ (db << 5))) = hA[db]; }); };
    sfor<0, 3>([&](auto i_) { qn[decltype(i_)::v][0] = bf8{}; qn[decltype(i_)::v][1] = bf8{}; });

    auto bar_a = [&](int c, bool second) { if ((((unsigned)WHOLE >> c) & 1) == (unsigned)second) { STAMP(6 + 4 * c); bar_v<0>(); } };
    auto w_and_c = [&](int c) { STAMP(5 + 4 * c); w_issue(c); STAMP(7 + 4 * c); bar_l(); };
#pragma clang loop unroll(disable)
    for (int it = 0; it < 2; ++it) {
      it = opqs(it);
      const int c0 = 12 * it;
      sfor<0, 6>([&](auto r_) { constexpr int r = decltype(r_)::v, ch = r >> 1, kb = r & 1;
        if constexpr (kb == 0) { bf8 n[2] = {qn[ch][0], qn[ch][1]}; if (it == 0) read_q(n, ch & 1); qc[0] = n[0]; qc[1] = n[1]; }
        k_unit(IC<kb>{}, IC<0>{}, IC<ch == 0>{});
        bar_a(c0 + r, false);
        k_unit(IC<kb>{}, IC<1>{}, IC<ch == 0>{});
        bar_a(c0 + r, true);
        if constexpr (r == 4) { max_half(IC<0>{}); sfor<0, 1>(exp_kk); }
        if constexpr (r == 5) sfor<1, 5>(exp_kk);
        w_and_c(c0 + r); });
      if (it == 0) read_q(qn[0], 1);
      v_unit(IC<0>{}, IC<0>{}, IC<1>{}); sfor<5, 8>(exp_kk);
      bar_a(c0 + 6, false);
      v_unit(IC<0>{}, IC<1>{}, IC<1>{});
      bar_a(c0 + 6, true);
      max_half(IC<1>{}); sfor<8, 12>(exp_kk);
      w_and_c(c0 + 6);
      v_unit(IC<1>{}, IC<0>{}, IC<1>{}); sfor<12, 16>(exp_kk);
      bar_a(c0 + 7, false);
      v_unit(IC<1>{}, IC<1>{}, IC<1>{}); linv = 1.0f / Ls[0]; finish_o();
      if (it == 1) write_o(1);
      bar_a(c0 + 7, true);
      w_and_c(c0 + 7);
#pragma clang loop unroll(disable)
      for (int ch = 1; ch < 3; ++ch) {
        ch = opqs(ch);
        const int c = c0 + 6 + 2 * ch;
        if (it == 0) { bf8 n[2]; read_q(n, (ch + 1) & 1); qn[1][0] = qn[2][0]; qn[1][1] = qn[2][1]; qn[2][0] = n[0]; qn[2][1] = n[1]; }
        v_unit(IC<0>{}, IC<0>{}, IC<0>{});
        bar_a(c, false);
        v_unit(IC<0>{}, IC<1>{}, IC<0>{});
        bar_a(c, true);
        if (it == 0) write_o((ch - 1) & 1);
        w_and_c(c);
        if (c == 22) w_triple();
        v_unit(IC<1>{}, IC<0>{}, IC<0>{});
        if (c == 22) bar_l(); else bar_a(c + 1, false);
        v_unit(IC<1>{}, IC<1>{}, IC<0>{}); finish_o();
        if (it == 1 || ch == 2) write_o((it + ch) & 1);
        if (c == 22) break;
        bar_a(c + 1, true);
        w_and_c(c + 1);
      }
    }
    bar_v<12>();
    bar_v<0>();
    bar_l();
    if (w == 0) {
      const int l = lane_id(); const u64 tag = epoch + 1;
      if (l == 0) __hip_atomic_store(sync + 32 * (1 + mem), tag, __ATOMIC_RELAXED, __HIP_MEMORY_SCOPE_AGENT);
      for (unsigned spins = 0; spins < (1u << 20); ++spins) {
        u64 f = tag; if (l < 16) f = __hip_atomic_load(sync + 32 * (1 + l), __ATOMIC_RELAXED, __HIP_MEMORY_SCOPE_AGENT);
        if (__all(f == tag)) break;
        __builtin_amdgcn_s_sleep(1); }
      if (l == 0 && mem == 0) __hip_atomic_store(sync, tag, __ATOMIC_RELAXED, __HIP_MEMORY_SCOPE_AGENT);
      __builtin_amdgcn_fence(__ATOMIC_ACQUIRE, "wavefront");
    }
    bar_l();
    reduce_slice(w);
    STAMP_FLUSH();
  } else {
    __builtin_amdgcn_s_setprio(PROJ_PRIO);
    const int lane = lane_id();
    STAMP_DECL; STAMP(0);
    asm volatile("v_writelane_b32 %0, %1, 62" : "+v"(st1) : "s"((unsigned)t_first));
    const int qh = w & 1, ah = w >> 1, h = lane >> 5, b16 = (lane >> 4) & 1, q4 = (lane & 15) >> 2, p = lane & 3;
    const unsigned row = 32 * qh + (lane & 31), f8 = (row >> 1) & 7;
    const unsigned qa0 = (8 * h + q4) * 128 + ((ah ^ (q4 >> 1)) << 6) + 32 * b16 + 8 * p;
    const unsigned oa0 = (8 * h + q4) * 256 + (((2 * ah) ^ q4) << 6) + 32 * b16 + 8 * p;
    const unsigned ha0 = row * 128 + ((h ^ f8) << 4) + HB;
    constexpr int PD = 6;
    const unsigned vK = v_k(lane), vV = (32 * w + (lane >> 3)) * 384 + (((lane & 7) ^ (((lane >> 4) & 3) << 1)) << 4);
    unsigned k_so = 2 * 49152, k_n = 1, k_vE = vK, k_vO = vK ^ 64; u4 k_srd = sK;
    auto kv_tile = [&]() { const unsigned lb = (k_n & 1) * KVS + w4k;
      dma4<true>(lb, k_vE, k_vO, k_srd, k_so, 3072); dma4<true>(lb + US, k_vE, k_vO, k_srd, k_so + 49152, 3072);
      ++k_n; k_so += (k_n & 1) ? 2 * 49152u : 128u - 2 * 49152u;
      if (k_n == 6 || k_n == 12 || k_n == 18) { const bool isV = k_n != 12;
        k_so = k_n == 6 ? 0 : ITEM; k_srd[0] = sel(isV, sV[0], sK[0]); k_srd[1] = sel(isV, sV[1], sK[1]); k_vE = sel(isV, vV, vK); k_vO = sel(isV, vV, vK ^ 64); } };
    auto seam = [&](bool kv) { STAMP(k_n >= 2 ? 4 * k_n - 3 : 2);
      if (((unsigned)WHOLE >> (k_n - 1)) & 1) bar_v<0>(); else bar_v<4>();
      STAMP(k_n >= 2 ? 4 * k_n - 2 : 1);
      if (kv) {
        if (k_n == 2 || k_n == 7 || k_n == 15) { const unsigned l = lane_id(), tl = b * 120 + w * 30 + l; const bool q = k_n == 2;
          const u4 st = {sel(q, sQ[0], sO[0]), sel(q, sQ[1], sO[1]), sel(q, sQ[2], sO[2]), sQ[3]};
          touch4((k_n & 1) * KVS + w4k, l < 30 ? sel(q, (tl / 3) * 12288 + (tl % 3) * 128, tl * 128) : OOB, st, q ? 384 : k_n == 7 ? 0 : 245760); }
        kv_tile(); STAMP(k_n >= 3 ? 4 * k_n - 5 : 2); bar_v<8>(); } else bar_v<0>();
      STAMPA(k_n >= 2 ? 4 * k_n - 4 : 2); };

#pragma clang loop unroll(disable)
    for (int pass = DRY ? 0 : 1; pass < 2; ++pass) {
    pass = opqs(pass);
    const bool dry = pass == 0;
    {
      bf8 xf[40]; f16v Qa = {};
      sfor<0, 40>([&](auto s_) { xf[decltype(s_)::v] = undef8(); });
      auto q_run = [&](auto t0_, auto n_, unsigned lb) { constexpr int T0 = decltype(t0_)::v, N = 8 * decltype(n_)::v;
        const unsigned a0 = opq(qa0 + lb);
        bf8 a[N];
        auto rd = [&](auto i_) { constexpr int i = decltype(i_)::v; a[i] = ldtr(sm, a0 + i * 2048, a0 + i * 2048 + 512); };
        sfor<0, PD>(rd); SB();
        sfor<0, N / 2>([&](auto j_) { constexpr int i = 2 * decltype(j_)::v;
          f16v c; if constexpr (T0 == 0 && i == 0) c = f16v{}; else c = Qa;
          c = mfma32(a[i + 1], xf[8 * T0 + i + 1], c); Qa = mfma32(a[i], xf[8 * T0 + i], c);
          if constexpr (i + PD < N) { rd(IC<i + PD>{}); rd(IC<i + PD + 1>{}); }
          SB(); }); };
      if (!dry) { bar_l();
        STAMP(105);
        const unsigned xa = row * 256 + ((h ^ (row & 15)) << 4);
        sfor<0, 40>([&](auto s_) { constexpr int s = decltype(s_)::v; xf[s] = ld128(sm, (xa ^ ((s & 7) << 5)) + (s >= 32 ? HB : (s >> 3) * US)); });
        bar_l();
        STAMP(106); }
#pragma clang loop unroll(disable)
      for (int gq = 0; gq < 6; ++gq) {
        gq = opqs(gq);
        q_run(IC<0>{}, IC<2>{}, XB);
        if (!dry) seam(gq > 0);
        q_run(IC<2>{}, IC<3>{}, YB);
        if (dry) break;
        { const unsigned a = opq((ha0 ^ (h << 4)) + (gq & 1) * 8192 + 8 * h) ^ (ah << 6);
          sfor<0, 4>([&](auto i_) { constexpr int i = decltype(i_)::v;
            *(LAS u2*)(sm + (a ^ (i << 4))) = u2{pk(Qa[4 * i], Qa[4 * i + 1]), pk(Qa[4 * i + 2], Qa[4 * i + 3])}; }); }
        seam(true);
      }
    }
    {
      f16v acc[10]; bf8 ob[4];
      sfor<0, 10>([&](auto i_) { acc[decltype(i_)::v] = f16v{}; });
      const auto rsP = __builtin_amdgcn_make_buffer_rsrc(part + (size_t)(b * 16 + mem) * 81920 + (qh * 40 + 4 * ah) * 1024, 0, dry ? 0 : 81920, 0x00020000);
      auto store_t = [&](auto t_) { constexpr int T = decltype(t_)::v; const unsigned pv = lane_id() * 16;
        sfor<0, 4>([&](auto i_) { constexpr int j = decltype(i_)::v >> 1, hf = decltype(i_)::v & 1; const f16v& a = acc[2 * T + j];
          u4 u = {pk(a[8 * hf], a[8 * hf + 1]), pk(a[8 * hf + 2], a[8 * hf + 3]), pk(a[8 * hf + 4], a[8 * hf + 5]), pk(a[8 * hf + 6], a[8 * hf + 7])};
          __builtin_amdgcn_raw_buffer_store_b128(u, rsP, pv, ((4 * T + j) * 2 + hf) * 1024, PART_AUX); }); };
      auto o_run = [&](auto t0_, auto n_, unsigned lb, bool last) { constexpr int T0 = decltype(t0_)::v, N = 8 * decltype(n_)::v;
        const unsigned a0 = opq(oa0 + lb), a1 = a0 ^ 64;
        bf8 a[N];
        auto rd = [&](auto i_) { constexpr int i = decltype(i_)::v; const unsigned ad = ((i & 4) ? a1 : a0) + (i >> 3) * US + (i & 3) * 4096; a[i] = ldtr(sm, ad, ad + 1024); };
        sfor<0, PD>(rd); SB();
        sfor<0, N / 2>([&](auto j_) { constexpr int j = decltype(j_)::v, i = 2 * j, blk = 2 * T0 + (j >> 1), k = (j & 1) * 2;
          f16v c = mfma32(a[i + 1], ob[k + 1], acc[blk]); acc[blk] = mfma32(a[i], ob[k], c);
          if constexpr (i + PD < N) { rd(IC<i + PD>{}); rd(IC<i + PD + 1>{}); }
          SB();
          if constexpr ((j & 3) == 3) { if (last) store_t(IC<T0 + (j >> 2)>{}); } }); };
#pragma clang loop unroll(disable)
      for (int go = dry ? 5 : 0; go < 6; ++go) {
        go = opqs(go);
        const bool last = go == 5;
        { const unsigned ad = opq(ha0 + (go & 1) * 8192); sfor<0, 4>([&](auto k_) { constexpr int k = decltype(k_)::v; ob[k] = ld128(sm, ad ^ (k << 5)); }); }
        o_run(IC<0>{}, IC<2>{}, XB, last);
        if (!last) seam(true); else if (!dry) bar_l();
        o_run(IC<2>{}, IC<3>{}, YB, last);
        if (last) break;
        seam(go != 4);
        if (go < 3) seam(true);
        if (go == 4) { STAMP(100); bar_l(); STAMP(101); }
      }
    }
    }
    STAMP(102);
    bar_v<0>();
    STAMP(103);
    bar_l();
    STAMP(124);
    if (w == 0) reduce_slice(4);
    asm volatile("s_waitcnt vmcnt(0)" ::: "memory"); STAMP(125);
    STAMP_FLUSH();
  }
}

__global__ void tiny_kernel(u64* st) { st[310000] = (unsigned)__builtin_amdgcn_s_memrealtime(); }
__global__ void spin_kernel(const u64* st, int mode) {
  const unsigned sp0 = (unsigned)__builtin_amdgcn_s_memrealtime();
  unsigned mn = 0xffffffffu, mx = 0, f0 = 0xffffffffu, f1 = 0, ka = 0; const unsigned ref = (unsigned)st[4 * 128];
  for (int i = 0; i < 256; ++i) { const u64* s = st + (i * 8 + 4) * 128; unsigned a = (unsigned)s[0] - ref + 100000, e = (unsigned)s[125] - ref + 100000, f = (unsigned)s[126] - ref + 100000;
    mn = a < mn ? a : mn; mx = e > mx ? e : mx; f0 = f < f0 ? f : f0; f1 = f > f1 ? f : f1; ka += a - f; }
  u64 T = mode == 0 ? (u64)(mx - mn) * 20 : mode == 1 ? (u64)ka : mode == 2 ? (u64)(f0 - ((unsigned)st[310000] - ref + 100000)) * 100 : mode == 3 ? (u64)((sp0 - ref + 100000) - mx) * 100 : mode == 6 ? (u64)(f1 - f0) * 100 : 0;
  if (T > 400000) T = 400000;
  u64 t0 = __builtin_amdgcn_s_memrealtime(); while (__builtin_amdgcn_s_memrealtime() - t0 < T) __builtin_amdgcn_s_sleep(8);
}
extern "C" void kernel_launch(void* const* d_in, const int* in_sizes, int n_in, void* d_out, int out_size, void* d_ws, size_t ws_size, hipStream_t stream) {

  fused_kernel<<<256, 512, 0, stream>>>((const char*)d_in[0], (const char*)d_in[1], (const char*)d_in[2], (const char*)d_in[3], (const char*)d_in[4], (char*)d_ws, (char*)d_out
                                        , (u64*)((char*)d_ws + (64 << 20))
  );
  spin_kernel<<<1, 64, 0, stream>>>((const u64*)((char*)d_ws + (64 << 20)), 4);
}
```

```cpp
#include <hip/hip_runtime.h>
#include <hip/hip_bf16.h>
#include <stdint.h>

typedef short s4 __attribute__((ext_vector_type(4)));
typedef short s8 __attribute__((ext_vector_type(8)));
typedef __bf16 bf8 __attribute__((ext_vector_type(8)));
typedef __bf16 bf2 __attribute__((ext_vector_type(2)));
typedef float f2 __attribute__((ext_vector_type(2)));
typedef float f4 __attribute__((ext_vector_type(4)));
typedef float f16v __attribute__((ext_vector_type(16)));
typedef unsigned u2 __attribute__((ext_vector_type(2)));
typedef unsigned u4 __attribute__((ext_vector_type(4)));
typedef unsigned long long u64;
#define LAS __attribute__((address_space(3)))
#define GAS __attribute__((address_space(1)))
#define DI __device__ __forceinline__

template <int N> struct IC { static constexpr int v = N; };
template <int A, int B, class F> DI void sfor(F&& f) { if constexpr (A < B) { f(IC<A>{}); sfor<A + 1, B>(f); } }

constexpr unsigned US = 16384, KVS = 32768, XB = 65536, YB = 98304, HB = 147456, LDS_BYTES = 163840;
constexpr unsigned CNT_LDS = 0;
constexpr unsigned PART_OFF = 1 << 20;
constexpr unsigned ITEM = 196608;
constexpr float SC = 0.07216878364870322f * 1.4426950408889634f;

#define PROJ_PRIO 0
#define ATT_PRIO 1
#define GATE_C 21
#define CODE_BYTES 30000u
#define DRY 0
#define PART_AUX 16
#define STAMP(i) do {} while (0)
#define STAMPA(i) do {} while (0)
#define STAMP_DECL
#define STAMP_FLUSH() do {} while (0)

#define NOPS
template <class T> DI T rfl(T v) { return v; }
DI u4 mk_srd(const void* p, unsigned bytes) {
  const u64 a = (u64)p;
  u4 r = {(unsigned)__builtin_amdgcn_readfirstlane((unsigned)a), (unsigned)__builtin_amdgcn_readfirstlane((unsigned)(a >> 32) & 0xffffu), bytes, 0x00020000u};
  return r;
}
template <bool NT> DI void dma4(unsigned mb, unsigned v0, unsigned v1, u4 srd, unsigned so, unsigned st) {
  unsigned t;
#define DMA4_BODY(POL) \
  asm volatile(NOPS "s_mov_b32 m0, %1\n\ts_add_u32 %0, %5, %6\n\tbuffer_load_dwordx4 %2, %4, %5 offen " POL "lds\n\t" \
               "s_add_u32 m0, %1, 0x400\n\ts_nop 0\n\tbuffer_load_dwordx4 %3, %4, %0 offen " POL "lds\n\t" \
               "s_add_u32 m0, %1, 0x800\n\ts_add_u32 %0, %0, %6\n\tbuffer_load_dwordx4 %2, %4, %0 offen " POL "lds\n\t" \
               "s_add_u32 m0, %1, 0xc00\n\ts_add_u32 %0, %0, %6\n\tbuffer_load_dwordx4 %3, %4, %0 offen " POL "lds" \
               : "=&s"(t) : "s"(rfl(mb)), "v"(v0), "v"(v1), "s"(rfl(srd)), "s"(rfl(so)), "s"(rfl(st)) : "memory", "scc")
  if constexpr (NT) DMA4_BODY("nt "); else DMA4_BODY("");
#undef DMA4_BODY
}
template <int J> DI void dma1nt(unsigned mb, unsigned voff, u4 srd, unsigned so) {
  unsigned t;
  asm volatile(NOPS "s_add_u32 m0, %1, %2\n\ts_add_u32 %0, %3, %4\n\tbuffer_load_dwordx4 %5, %6, %0 offen nt lds"
               : "=&s"(t) : "s"(rfl(mb)), "i"(J * 1024), "s"(rfl(so)), "i"(J * 3072), "v"(voff), "s"(rfl(srd)) : "memory", "scc");
}
DI void dma1(unsigned m0v, unsigned voff, u4 srd, unsigned so) {
  asm volatile("s_mov_b32 m0, %0\n\ts_nop 0\n\tbuffer_load_dwordx4 %1, %2, %3 offen lds" :: "s"(m0v), "v"(voff), "s"(srd), "s"(so) : "memory");
}
DI void touch4(unsigned m0v, unsigned voff, u4 srd, unsigned so) {
  asm volatile(NOPS "s_mov_b32 m0, %0\n\ts_nop 0\n\tbuffer_load_dword %1, %2, %3 offen lds" :: "s"(rfl(m0v)), "v"(voff), "s"(rfl(srd)), "s"(rfl(so)) : "memory");
}
#define SB() __builtin_amdgcn_sched_barrier(0)
template <int N> DI void bar_v() { SB(); asm volatile("s_waitcnt vmcnt(%0) lgkmcnt(0)\n\ts_barrier" ::"i"(N) : "memory"); SB(); }
DI void bar_l() { SB(); asm volatile("s_waitcnt lgkmcnt(0)\n\ts_barrier" ::: "memory"); SB(); }

DI bf8 ld128(LAS char* sm, unsigned a) { return __builtin_bit_cast(bf8, *(LAS s8*)(sm + a)); }
DI bf8 ldtr(LAS char* sm, unsigned a0, unsigned a1) {
  s4 x = __builtin_amdgcn_ds_read_tr16_b64_v4i16((LAS s4*)(sm + a0)), y = __builtin_amdgcn_ds_read_tr16_b64_v4i16((LAS s4*)(sm + a1));
  return __builtin_bit_cast(bf8, __builtin_shufflevector(x, y, 0, 1, 2, 3, 4, 5, 6, 7));
}
DI unsigned opq(unsigned v) { asm volatile("" : "+v"(v)); return v; }
DI bf8 undef8() { bf8 v; asm volatile("" : "=v"(v)); return v; }
DI unsigned sel(bool c, unsigned a, unsigned b) { return c ? a : b; }
DI int opqs(int v) { v = __builtin_amdgcn_readfirstlane(v); asm volatile("" : "+s"(v)); return v; }
DI int lane_id() { unsigned z = 0; asm volatile("" : "+v"(z)); return __builtin_amdgcn_mbcnt_hi(~0u, __builtin_amdgcn_mbcnt_lo(~0u, z)); }
DI unsigned pk(float a, float b) { f2 x = {a, b}; return __builtin_bit_cast(unsigned, __builtin_convertvector(x, bf2)); }
DI f4 mfma16(bf8 a, bf8 b, f4 c) { return __builtin_amdgcn_mfma_f32_16x16x32_bf16(a, b, c, 0, 0, 0); }
DI f16v mfma32(bf8 a, bf8 b, f16v c) { return __builtin_amdgcn_mfma_f32_32x32x16_bf16(a, b, c, 0, 0, 0); }

constexpr u64 w_table() { u64 t = 0; for (int c = 0; c < 24; ++c) { const int n = c < 12 ? 1 + (c & 1) : (c == 13 || c == 16 || c == 19 || c == 21 || c == 23) ? 1 : (c == 14 || c == 17 || c == 20 || c == 22) ? 2 : 0; t |= (u64)n << (2 * c); } return t; }
#define WHOLE ((1u<<19)|(1u<<21))

__global__ __launch_bounds__(512, 2) void fused_kernel(const char* __restrict__ x, const char* __restrict__ K, const char* __restrict__ V,
                                                       const char* __restrict__ wq, const char* __restrict__ wo, char* ws, char* __restrict__ out
) {
  __shared__ __attribute__((aligned(16))) char smem[LDS_BYTES];
  LAS char* sm = (LAS char*)smem;
  const int wave = __builtin_amdgcn_readfirstlane(threadIdx.x >> 6), w = wave & 3;
  const int xcd = blockIdx.x & 7, idx = blockIdx.x >> 3, b = idx >> 1, hp = idx & 1, h0 = xcd * 4 + hp * 2, mem = xcd * 2 + hp;
  const u4 sK = mk_srd(K + (size_t)(b * 32 + h0) * ITEM, 2 * ITEM), sV = mk_srd(V + (size_t)(b * 32 + h0) * ITEM, 2 * ITEM);
  const u4 sQ = mk_srd(wq + h0 * 384, 640 * 12288 - h0 * 384), sO = mk_srd(wo + h0 * (192 * 1280), 384 * 1280);
  char* const part = ws + PART_OFF;
  GAS u64* const sync = (GAS u64*)(ws + b * 1024);
  const unsigned w4k = w * 4096u, OOB = 0x7ffffff0u;
  auto v_k = [&](int l) { return (unsigned)((32 * w + (l >> 3)) * 384 + (((l & 7) ^ (l >> 4)) << 4)); };

  if (wave < 4) {
    __builtin_amdgcn_s_setprio(ATT_PRIO);
    const int lane = lane_id();
    STAMP_DECL; STAMP(0);
    const int r16 = lane & 15, g = lane >> 4, q4 = (lane & 15) >> 2, p = lane & 3;
    unsigned w_so = 0, w_g = 0, w_v = (32 * w + (lane >> 3)) * 12288 + (((lane & 7) ^ (((lane >> 4) & 1) << 2)) << 4), w_st = 8 * 12288, w_dT = 128 * 12288, w_dG = 128u - 5u * 128 * 12288;
    u4 w_srd = sQ;
    auto w_tile = [&](unsigned lb) { dma4<false>(lb + w4k, w_v, w_v, w_srd, w_so, w_st); w_so += w_dT; };
    auto w_pair = [&]() { w_tile(XB); w_tile(XB + US); };
    auto w_triple = [&]() { w_tile(YB); w_tile(YB + US); w_tile(YB + 2 * US); w_so += w_dG;
      if (++w_g == 6) { const int l = lane_id(); w_so = 0; w_v = (16 * w + (l >> 4)) * 1280 + (((l & 15) ^ ((l >> 4) << 2)) << 4); w_st = 4 * 1280; w_dT = 256; w_dG = 64 * 1280 - 5 * 256; w_srd = sO; } };
    auto w_issue = [&](int c) { const unsigned n = (unsigned)(w_table() >> (2 * c + 2)) & 3; if (n & 1) w_pair(); if (n & 2) w_triple(); };
    const u64 epoch = __hip_atomic_load(sync, __ATOMIC_RELAXED, __HIP_MEMORY_SCOPE_AGENT);
    { const unsigned trow = (b * 40 + w * 10) * 12288;
      bar_l();
      w_pair();
      STAMP(104);
      bar_l();
      STAMP(105);
      w_triple();
      bar_v<12>();
      STAMP(106);
      touch4(w4k, lane < 20 ? trow + (lane >> 1) * 12288 + (lane & 1) * 128 : OOB, sQ, 128);
      { const unsigned vK = v_k(lane); for (int u = 0; u < 2; ++u) dma4<true>(u * US + w4k, vK, vK ^ 64, sK, u * 49152, 3072); }
      STAMP(107);
      bar_v<9>();
      bar_l();
      w_pair();
      bar_v<12>();
      STAMP(1); }
    const unsigned ka0 = r16 * 128 + ((g ^ ((r16 >> 1) & 7)) << 4);
    const unsigned va0 = (4 * g + q4) * 128 + p * 8 + (((2 * g + (q4 >> 1)) & 3) << 5);
    const unsigned wa0 = (16 * w + r16) * 128 + (((g >> 1) ^ ((r16 >> 1) & 7)) << 4) + (g & 1) * 8 + HB;
    bf8 qc[2], qn[3][2], pf[16]; f4 S[32], O[4], Ls; u2 hA[4]; float m0, mc[2], alpha, linv;
    const u4 ones_u = {0x3f803f80u, 0x3f803f80u, 0x3f803f80u, 0x3f803f80u};
    auto read_q = [&](bf8* q, int hs) { const unsigned a = opq(ka0 + HB + w * 2048 + hs * 8192); q[0] = ld128(sm, a); q[1] = ld128(sm, a ^ 64); };
    auto k_unit = [&](auto kb_, auto un_, auto first_) { constexpr int kb = decltype(kb_)::v, un = decltype(un_)::v, PD = 8; constexpr bool first = decltype(first_)::v;
      const unsigned a0 = opq(ka0) + kb * KVS + un * US, a1 = a0 ^ 64;
      bf8 a[16];
      auto rd = [&](auto i_) { constexpr int i = decltype(i_)::v; a[i] = ld128(sm, ((i & 1) ? a1 : a0) + (i >> 1) * 2048); };
      sfor<0, PD>(rd); SB();
      sfor<0, 8>([&](auto j_) { constexpr int j = decltype(j_)::v, blk = 16 * kb + 8 * un + j;
        f4 c; if constexpr (first) c = f4{0, 0, 0, 0}; else c = S[blk];
        c = mfma16(a[2 * j + 1], qc[1], c); S[blk] = mfma16(a[2 * j], qc[0], c);
        if constexpr (2 * j + PD < 16) { rd(IC<2 * j + PD>{}); rd(IC<2 * j + PD + 1>{}); }
        SB(); }); };
    auto max_half = [&](auto h_) { constexpr int h = decltype(h_)::v;
      float m = fmaxf(S[16 * h][0], S[16 * h][1]), m2 = fmaxf(S[16 * h][2], S[16 * h][3]);
      sfor<1, 16>([&](auto b_) { constexpr int i = 16 * h + decltype(b_)::v; m = fmaxf(fmaxf(m, S[i][0]), S[i][1]); m2 = fmaxf(fmaxf(m2, S[i][2]), S[i][3]); });
      m = fmaxf(m, m2);
      { auto r = __builtin_amdgcn_permlane16_swap(__float_as_uint(m), __float_as_uint(m), false, false); m = fmaxf(__uint_as_float(r[0]), __uint_as_float(r[1])); }
      { auto r = __builtin_amdgcn_permlane32_swap(__float_as_uint(m), __float_as_uint(m), false, false); m = fmaxf(__uint_as_float(r[0]), __uint_as_float(r[1])); }
      if constexpr (h == 0) m0 = m; else { m = fmaxf(m, m0); alpha = __builtin_amdgcn_exp2f((m0 - m) * SC); }
      mc[h] = -m * SC; };
    auto exp_kk = [&](auto kk_) { constexpr int kk = decltype(kk_)::v; float e[8];
      sfor<0, 8>([&](auto j_) { constexpr int j = decltype(j_)::v; e[j] = __builtin_amdgcn_exp2f(fmaf(S[2 * kk + (j >> 2)][j & 3], SC, mc[kk >> 3])); });
      u4 u = {pk(e[0], e[1]), pk(e[2], e[3]), pk(e[4], e[5]), pk(e[6], e[7])}; pf[kk] = __builtin_bit_cast(bf8, u); };
    auto v_unit = [&](auto kb_, auto un_, auto ls_) { constexpr int kb = decltype(kb_)::v, un = decltype(un_)::v, PD = 6; constexpr bool ls = decltype(ls_)::v;
      const unsigned a0 = opq(va0) + kb * KVS + un * US;
      bf8 a[16];
      auto rd = [&](auto i_) { constexpr int i = decltype(i_)::v; const unsigned ad = (a0 ^ ((i & 3) << 5)) + (i >> 2) * 4096; a[i] = ldtr(sm, ad, ad + 2048); };
      sfor<0, PD>(rd);
      if constexpr (kb == 1 && un == 0) { sfor<0, 4>([&](auto d_) { constexpr int db = decltype(d_)::v; O[db] *= alpha; }); if constexpr (ls) Ls *= alpha; }
      SB();
      sfor<0, 8>([&](auto j_) { constexpr int j = decltype(j_)::v, kk = 8 * kb + 4 * un + (j >> 1), d0 = (j & 1) * 2;
        f4 c0, c1; if constexpr (kk == 0) { c0 = f4{0, 0, 0, 0}; c1 = c0; } else { c0 = O[d0]; c1 = O[d0 + 1]; }
        O[d0 + 1] = mfma16(a[2 * j + 1], pf[kk], c1); O[d0] = mfma16(a[2 * j], pf[kk], c0);
        if constexpr (ls && (j & 1)) { f4 cl; if constexpr (kk == 0) cl = f4{0, 0, 0, 0}; else cl = Ls; Ls = mfma16(__builtin_bit_cast(bf8, ones_u), pf[kk], cl); }
        if constexpr (2 * j + PD < 16) { rd(IC<2 * j + PD>{}); rd(IC<2 * j + PD + 1>{}); }
        SB(); }); };
    auto finish_o = [&]() { sfor<0, 4>([&](auto d_) { constexpr int db = decltype(d_)::v;
        hA[db] = u2{pk(O[db][0] * linv, O[db][1] * linv), pk(O[db][2] * linv, O[db][3] * linv)}; }); };
    auto write_o = [&](int hs) { const unsigned a = opq(wa0 + hs * 8192);
      sfor<0, 4>([&](auto d_) { constexpr int db = decltype(d_)::v; *(LAS u2*)(sm + (a ^ (db << 5))) = hA[db]; }); };
    sfor<0, 3>([&](auto i_) { qn[decltype(i_)::v][0] = bf8{}; qn[decltype(i_)::v][1] = bf8{}; });

    auto bar_a = [&](int c, bool second) { if ((((unsigned)WHOLE >> c) & 1) == (unsigned)second) { STAMP(6 + 4 * c); bar_v<0>(); } };
    auto w_and_c = [&](int c) { STAMP(5 + 4 * c); w_issue(c); STAMP(7 + 4 * c); bar_l(); };
#pragma clang loop unroll(disable)
    for (int it = 0; it < 2; ++it) {
      it = opqs(it);
      const int c0 = 12 * it;
      sfor<0, 6>([&](auto r_) { constexpr int r = decltype(r_)::v, ch = r >> 1, kb = r & 1;
        if constexpr (kb == 0) { bf8 n[2] = {qn[ch][0], qn[ch][1]}; if (it == 0) read_q(n, ch & 1); qc[0] = n[0]; qc[1] = n[1]; }
        k_unit(IC<kb>{}, IC<0>{}, IC<ch == 0>{});
        if constexpr (r == 5) sfor<1, 4>(exp_kk);
        bar_a(c0 + r, false);
        k_unit(IC<kb>{}, IC<1>{}, IC<ch == 0>{});
        bar_a(c0 + r, true);
        if constexpr (r == 4) { max_half(IC<0>{}); sfor<0, 1>(exp_kk); }
        if constexpr (r == 5) { sfor<4, 5>(exp_kk); max_half(IC<1>{}); }
        w_and_c(c0 + r); });
      if (it == 0) read_q(qn[0], 1);
      v_unit(IC<0>{}, IC<0>{}, IC<1>{}); sfor<5, 9>(exp_kk);
      bar_a(c0 + 6, false);
      v_unit(IC<0>{}, IC<1>{}, IC<1>{});
      bar_a(c0 + 6, true);
      sfor<9, 12>(exp_kk);
      w_and_c(c0 + 6);
      v_unit(IC<1>{}, IC<0>{}, IC<1>{}); sfor<12, 16>(exp_kk);
      bar_a(c0 + 7, false);
      v_unit(IC<1>{}, IC<1>{}, IC<1>{}); linv = 1.0f / Ls[0]; finish_o();
      if (it == 1) write_o(1);
      bar_a(c0 + 7, true);
      w_and_c(c0 + 7);
#pragma clang loop unroll(disable)
      for (int ch = 1; ch < 3; ++ch) {
        ch = opqs(ch);
        const int c = c0 + 6 + 2 * ch;
        if (it == 0) { bf8 n[2]; read_q(n, (ch + 1) & 1); qn[1][0] = qn[2][0]; qn[1][1] = qn[2][1]; qn[2][0] = n[0]; qn[2][1] = n[1]; }
        v_unit(IC<0>{}, IC<0>{}, IC<0>{});
        bar_a(c, false);
        v_unit(IC<0>{}, IC<1>{}, IC<0>{});
        bar_a(c, true);
        if (it == 0) write_o((ch - 1) & 1);
        w_and_c(c);
        if (c == 22) w_triple();
        v_unit(IC<1>{}, IC<0>{}, IC<0>{});
        if (c == 22) bar_l(); else bar_a(c + 1, false);
        v_unit(IC<1>{}, IC<1>{}, IC<0>{}); finish_o();
        if (it == 1 || ch == 2) write_o((it + ch) & 1);
        if (c == 22) break;
        bar_a(c + 1, true);
        w_and_c(c + 1);
      }
    }
    if (w == 0) *(LAS unsigned*)(sm + CNT_LDS) = 0;
    bar_v<12>();
    bar_v<0>();
    STAMP(120);
    {
      const int l = lane_id(); const u64 tag = epoch + 1;
      auto where = [&](int i, int r, unsigned& vo, unsigned& dst) { const int pi = 5 * mem + i, qh = pi / 40, L = (pi % 40) >> 2, o = pi & 3, un = r >> 4, h = (r >> 3) & 1, q8 = r & 7, nblk = 2 * L + (un >> 1), hf = un & 1;
        vo = ((qh * 20 + nblk) * 2 + hf) * 1024 + (32 * h + 8 * o + q8) * 16; dst = (32 * qh + 8 * o + q8) * 1280 + (32 * nblk + 16 * hf + 4 * h) * 2; };
      unsigned vo0, d0, vo1, d1; where(w, l, vo0, d0); where(4, 16 * w + (l & 15), vo1, d1); if (l >= 16) vo1 = OOB;
      const auto rs = __builtin_amdgcn_make_buffer_rsrc(part + (size_t)b * 16 * 81920, 0, 16 * 81920, 0x00020000);
      u4 v0[16], v1[16]; sfor<0, 16>([&](auto p_) { v0[decltype(p_)::v] = u4{0, 0, 0, 0}; v1[decltype(p_)::v] = u4{0, 0, 0, 0}; });
      unsigned done = 0;
      for (unsigned spins = 0; spins < (1u << 20); ++spins) {
        u64 f = 0; if (l < 16) f = __hip_atomic_load(sync + 32 + l, __ATOMIC_RELAXED, __HIP_MEMORY_SCOPE_AGENT);
        const unsigned fresh = (unsigned)__ballot(l < 16 && f == tag) & ~done;
        __builtin_amdgcn_fence(__ATOMIC_ACQUIRE, "wavefront");
        sfor<0, 16>([&](auto p_) { constexpr int p = decltype(p_)::v;
          if (fresh & (1u << p)) { v0[p] = __builtin_amdgcn_raw_buffer_load_b128(rs, vo0, p * 81920, 16); v1[p] = __builtin_amdgcn_raw_buffer_load_b128(rs, vo1, p * 81920, 16); } });
        done |= fresh;
        if (done == 0xffffu) break;
        if (!fresh) __builtin_amdgcn_s_sleep(1); }
      STAMP(124);
      if (w == 0 && mem == 0 && l == 0 && done == 0xffffu) __hip_atomic_store(sync, tag, __ATOMIC_RELAXED, __HIP_MEMORY_SCOPE_AGENT);
      auto fin = [&](const u4* v, unsigned d) { float a[8] = {};
        sfor<0, 16>([&](auto p_) { constexpr int p = decltype(p_)::v; sfor<0, 4>([&](auto e_) { constexpr int e = decltype(e_)::v;
          a[2 * e] += __uint_as_float(v[p][e] << 16); a[2 * e + 1] += __uint_as_float(v[p][e] & 0xffff0000u); }); });
        char* dst = out + (size_t)b * 81920 + d;
        *(u2*)dst = u2{pk(a[0], a[1]), pk(a[2], a[3])}; *(u2*)(dst + 16) = u2{pk(a[4], a[5]), pk(a[6], a[7])}; };
      fin(v0, d0); if (l < 16) fin(v1, d1); }
    STAMP_FLUSH();
  } else {
    __builtin_amdgcn_s_setprio(PROJ_PRIO);
    const int lane = lane_id();
    STAMP_DECL; STAMP(0);
    const u64 epoch = __hip_atomic_load(sync, __ATOMIC_RELAXED, __HIP_MEMORY_SCOPE_AGENT);
    { const u4 sX = mk_srd(x + b * 81920, 81920);
      const unsigned vX = (16 * w + (lane >> 4)) * 1280 + (((lane & 15) ^ (lane >> 4)) << 4);
      const unsigned t0 = (hp ? HB : 0) + w4k;
      touch4(t0, lane < 10 ? (b * 40 + w * 10 + lane) * 12288 : OOB, sQ, 0);
      { u64 pc; asm volatile("s_getpc_b64 %0" : "=s"(pc));
        const u4 sC = {(unsigned)pc, (unsigned)(pc >> 32) & 0xffffu, CODE_BYTES, 0x00020000u};
        const u4 st = {w == 0 ? sK[0] : w == 1 ? sV[0] : w == 2 ? sO[0] : sC[0], w == 0 ? sK[1] : w == 1 ? sV[1] : w == 2 ? sO[1] : sC[1], w == 3 ? sC[2] : sK[2], sK[3]};
        touch4(t0, w == 3 ? (lane < 8 ? (idx * 8 + lane) * 128 : OOB) : lane < 2 ? lane * ITEM : OOB, st, 0); }
      for (int t = 0; t < 5; ++t) { const unsigned tt = hp ? 4 - t : t, lb = tt == 4 ? HB : tt * US;
        sfor<0, 4>([&](auto j_) { constexpr int j = decltype(j_)::v; dma1(lb + w4k + j * 1024, vX ^ (j << 6), sX, tt * 256 + j * 4 * 1280); }); }
      bar_l(); }
    const int qh = w & 1, ah = w >> 1, h = lane >> 5, b16 = (lane >> 4) & 1, q4 = (lane & 15) >> 2, p = lane & 3;
    const unsigned row = 32 * qh + (lane & 31), f8 = (row >> 1) & 7;
    const unsigned qa0 = (8 * h + q4) * 128 + ((ah ^ (q4 >> 1)) << 6) + 32 * b16 + 8 * p;
    const unsigned oa0 = (8 * h + q4) * 256 + (((2 * ah) ^ q4) << 6) + 32 * b16 + 8 * p;
    const unsigned ha0 = row * 128 + ((h ^ f8) << 4) + HB;
    constexpr int PD = 6;
    const unsigned vK = v_k(lane), vV = (32 * w + (lane >> 3)) * 384 + (((lane & 7) ^ (((lane >> 4) & 3) << 1)) << 4);
    unsigned k_so = 2 * 49152, k_n = 1, k_vE = vK, k_vO = vK ^ 64; u4 k_srd = sK;
    auto kv_tile = [&]() { const unsigned lb = (k_n & 1) * KVS + w4k;
      dma4<true>(lb, k_vE, k_vO, k_srd, k_so, 3072); dma4<true>(lb + US, k_vE, k_vO, k_srd, k_so + 49152, 3072);
      ++k_n; k_so += (k_n & 1) ? 2 * 49152u : 128u - 2 * 49152u;
      if (k_n == 6 || k_n == 12 || k_n == 18) { const bool isV = k_n != 12;
        k_so = k_n == 6 ? 0 : ITEM; k_srd[0] = sel(isV, sV[0], sK[0]); k_srd[1] = sel(isV, sV[1], sK[1]); k_vE = sel(isV, vV, vK); k_vO = sel(isV, vV, vK ^ 64); } };
    auto seam = [&](bool kv) { STAMP(k_n >= 2 ? 4 * k_n - 3 : 2);
      if (((unsigned)WHOLE >> (k_n - 1)) & 1) bar_v<0>(); else bar_v<4>();
      STAMP(k_n >= 2 ? 4 * k_n - 2 : 1);
      if (kv) {
        if (k_n == 2 || k_n == 7 || k_n == 15) { const unsigned l = lane_id(), tl = b * 120 + w * 30 + l; const bool q = k_n == 2;
          const u4 st = {sel(q, sQ[0], sO[0]), sel(q, sQ[1], sO[1]), sel(q, sQ[2], sO[2]), sQ[3]};
          touch4((k_n & 1) * KVS + w4k, l < 30 ? sel(q, (tl / 3) * 12288 + (tl % 3) * 128, tl * 128) : OOB, st, q ? 384 : k_n == 7 ? 0 : 245760); }
        kv_tile(); STAMP(k_n >= 3 ? 4 * k_n - 5 : 2); bar_v<8>(); } else bar_v<0>();
      STAMPA(k_n >= 2 ? 4 * k_n - 4 : 2); };

#pragma clang loop unroll(disable)
    for (int pass = DRY ? 0 : 1; pass < 2; ++pass) {
    pass = opqs(pass);
    const bool dry = pass == 0;
    {
      bf8 xf[40]; f16v Qa = {};
      sfor<0, 40>([&](auto s_) { xf[decltype(s_)::v] = undef8(); });
      auto q_run = [&](auto t0_, auto n_, unsigned lb) { constexpr int T0 = decltype(t0_)::v, N = 8 * decltype(n_)::v;
        const unsigned a0 = opq(qa0 + lb);
        bf8 a[N];
        auto rd = [&](auto i_) { constexpr int i = decltype(i_)::v; a[i] = ldtr(sm, a0 + i * 2048, a0 + i * 2048 + 512); };
        sfor<0, PD>(rd); SB();
        sfor<0, N / 2>([&](auto j_) { constexpr int i = 2 * decltype(j_)::v;
          f16v c; if constexpr (T0 == 0 && i == 0) c = f16v{}; else c = Qa;
          c = mfma32(a[i + 1], xf[8 * T0 + i + 1], c); Qa = mfma32(a[i], xf[8 * T0 + i], c);
          if constexpr (i + PD < N) { rd(IC<i + PD>{}); rd(IC<i + PD + 1>{}); }
          SB(); }); };
      if (!dry) { bar_v<0>();
        STAMP(105);
        const unsigned xa = row * 256 + ((h ^ (row & 15)) << 4);
        sfor<0, 40>([&](auto s_) { constexpr int s = decltype(s_)::v; xf[s] = ld128(sm, (xa ^ ((s & 7) << 5)) + (s >= 32 ? HB : (s >> 3) * US)); });
        bar_l();
        STAMP(106); }
#pragma clang loop unroll(disable)
      for (int gq = 0; gq < 6; ++gq) {
        gq = opqs(gq);
        q_run(IC<0>{}, IC<2>{}, XB);
        if (!dry) seam(gq > 0);
        q_run(IC<2>{}, IC<3>{}, YB);
        if (dry) break;
        { const unsigned a = opq((ha0 ^ (h << 4)) + (gq & 1) * 8192 + 8 * h) ^ (ah << 6);
          sfor<0, 4>([&](auto i_) { constexpr int i = decltype(i_)::v;
            *(LAS u2*)(sm + (a ^ (i << 4))) = u2{pk(Qa[4 * i], Qa[4 * i + 1]), pk(Qa[4 * i + 2], Qa[4 * i + 3])}; }); }
        seam(true);
      }
    }
    {
      f16v acc[10]; bf8 ob[4];
      sfor<0, 10>([&](auto i_) { acc[decltype(i_)::v] = f16v{}; });
      const unsigned pv = lane_id() * 16;
      const auto rsP = __builtin_amdgcn_make_buffer_rsrc(part + (size_t)(b * 16 + mem) * 81920 + (qh * 40 + 4 * ah) * 1024, 0, dry ? 0 : 81920, 0x00020000);
      auto store_u = [&](auto u_) { constexpr int u = decltype(u_)::v, blk = u >> 1, hf = u & 1; const f16v& a = acc[blk];
        const u4 v = {pk(a[8 * hf], a[8 * hf + 1]), pk(a[8 * hf + 2], a[8 * hf + 3]), pk(a[8 * hf + 4], a[8 * hf + 5]), pk(a[8 * hf + 6], a[8 * hf + 7])};
        __builtin_amdgcn_raw_buffer_store_b128(v, rsP, pv, ((4 * (blk >> 1) + (blk & 1)) * 2 + hf) * 1024, PART_AUX); };
      auto o_run = [&](auto t0_, auto n_, unsigned lb, bool last) { constexpr int T0 = decltype(t0_)::v, N = 8 * decltype(n_)::v;
        const unsigned a0 = opq(oa0 + lb), a1 = a0 ^ 64;
        bf8 a[N];
        auto rd = [&](auto i_) { constexpr int i = decltype(i_)::v; const unsigned ad = ((i & 4) ? a1 : a0) + (i >> 3) * US + (i & 3) * 4096; a[i] = ldtr(sm, ad, ad + 1024); };
        sfor<0, PD>(rd); SB();
        sfor<0, N / 2>([&](auto j_) { constexpr int j = decltype(j_)::v, i = 2 * j, blk = 2 * T0 + (j >> 1), k = (j & 1) * 2;
          f16v c = mfma32(a[i + 1], ob[k + 1], acc[blk]); acc[blk] = mfma32(a[i], ob[k], c);
          if constexpr (i + PD < N) { rd(IC<i + PD>{}); rd(IC<i + PD + 1>{}); }
          if constexpr (4 * T0 + j >= 2) { if (last) store_u(IC<4 * T0 + j - 2>{}); }
          SB();
          }); };
#pragma clang loop unroll(disable)
      for (int go = dry ? 5 : 0; go < 6; ++go) {
        go = opqs(go);
        const bool last = go == 5;
        { const unsigned ad = opq(ha0 + (go & 1) * 8192); sfor<0, 4>([&](auto k_) { constexpr int k = decltype(k_)::v; ob[k] = ld128(sm, ad ^ (k << 5)); }); }
        o_run(IC<0>{}, IC<2>{}, XB, last);
        if (!last) seam(true); else if (!dry) bar_l();
        o_run(IC<2>{}, IC<3>{}, YB, last);
        if (last) break;
        seam(go != 4);
        if (go < 3) seam(true);
        if (go == 4) { STAMP(100); bar_l(); STAMP(101); }
      }
      sfor<18, 20>(store_u);
    }
    }
    STAMP(102);
    asm volatile("s_waitcnt vmcnt(0)" ::: "memory");
    STAMP(103);
    if (lane_id() == 0) { const unsigned old = __hip_atomic_fetch_add((LAS unsigned*)(sm + CNT_LDS), 1u, __ATOMIC_RELAXED, __HIP_MEMORY_SCOPE_WORKGROUP);
      if (old == 3) __hip_atomic_store(sync + 32 + mem, epoch + 1, __ATOMIC_RELAXED, __HIP_MEMORY_SCOPE_AGENT); }
    STAMP_FLUSH();
  }
}

extern "C" void kernel_launch(void* const* d_in, const int* in_sizes, int n_in, void* d_out, int out_size, void* d_ws, size_t ws_size, hipStream_t stream) {
  fused_kernel<<<256, 512, 0, stream>>>((const char*)d_in[0], (const char*)d_in[1], (const char*)d_in[2], (const char*)d_in[3], (const char*)d_in[4], (char*)d_ws, (char*)d_out
  );
}
```

```cpp
#include <hip/hip_runtime.h>
#include <hip/hip_bf16.h>
#include <stdint.h>

typedef short s4 __attribute__((ext_vector_type(4)));
typedef short s8 __attribute__((ext_vector_type(8)));
typedef __bf16 bf8 __attribute__((ext_vector_type(8)));
typedef __bf16 bf2 __attribute__((ext_vector_type(2)));
typedef float f2 __attribute__((ext_vector_type(2)));
typedef float f4 __attribute__((ext_vector_type(4)));
typedef float f16v __attribute__((ext_vector_type(16)));
typedef unsigned u2 __attribute__((ext_vector_type(2)));
typedef unsigned u4 __attribute__((ext_vector_type(4)));
typedef unsigned long long u64;
#define LAS __attribute__((address_space(3)))
#define GAS __attribute__((address_space(1)))
#define DI __device__ __forceinline__

template <int N> struct IC { static constexpr int v = N; };
template <int A, int B, class F> DI void sfor(F&& f) { if constexpr (A < B) { f(IC<A>{}); sfor<A + 1, B>(f); } }

constexpr unsigned US = 16384, KVS = 32768, XB = 65536, YB = 98304, HB = 147456, LDS_BYTES = 163840;
constexpr unsigned PART_OFF = 1 << 20;
constexpr unsigned ITEM = 196608;
constexpr float SC = 0.07216878364870322f * 1.4426950408889634f;

#define PROJ_PRIO 0
#define ATT_PRIO 1
#define GATE_C 21
#define CODE_BYTES 30000u
#define DRY 0
#define PART_AUX 16
#define STAMP(i) do {} while (0)
#define STAMPA(i) do {} while (0)
#define STAMP_DECL
#define STAMP_FLUSH() do {} while (0)

#define NOPS
template <class T> DI T rfl(T v) { return v; }
DI u4 mk_srd(const void* p, unsigned bytes) {
  const u64 a = (u64)p;
  u4 r = {(unsigned)__builtin_amdgcn_readfirstlane((unsigned)a), (unsigned)__builtin_amdgcn_readfirstlane((unsigned)(a >> 32) & 0xffffu), bytes, 0x00020000u};
  return r;
}
template <bool NT> DI void dma4(unsigned mb, unsigned v0, unsigned v1, u4 srd, unsigned so, unsigned st) {
  unsigned t;
#define DMA4_BODY(POL) \
  asm volatile(NOPS "s_mov_b32 m0, %1\n\ts_add_u32 %0, %5, %6\n\tbuffer_load_dwordx4 %2, %4, %5 offen " POL "lds\n\t" \
               "s_add_u32 m0, %1, 0x400\n\ts_nop 0\n\tbuffer_load_dwordx4 %3, %4, %0 offen " POL "lds\n\t" \
               "s_add_u32 m0, %1, 0x800\n\ts_add_u32 %0, %0, %6\n\tbuffer_load_dwordx4 %2, %4, %0 offen " POL "lds\n\t" \
               "s_add_u32 m0, %1, 0xc00\n\ts_add_u32 %0, %0, %6\n\tbuffer_load_dwordx4 %3, %4, %0 offen " POL "lds" \
               : "=&s"(t) : "s"(rfl(mb)), "v"(v0), "v"(v1), "s"(rfl(srd)), "s"(rfl(so)), "s"(rfl(st)) : "memory", "scc")
  if constexpr (NT) DMA4_BODY("nt "); else DMA4_BODY("");
#undef DMA4_BODY
}
template <int J> DI void dma1nt(unsigned mb, unsigned voff, u4 srd, unsigned so) {
  unsigned t;
  asm volatile(NOPS "s_add_u32 m0, %1, %2\n\ts_add_u32 %0, %3, %4\n\tbuffer_load_dwordx4 %5, %6, %0 offen nt lds"
               : "=&s"(t) : "s"(rfl(mb)), "i"(J * 1024), "s"(rfl(so)), "i"(J * 3072), "v"(voff), "s"(rfl(srd)) : "memory", "scc");
}
DI void dma1(unsigned m0v, unsigned voff, u4 srd, unsigned so) {
  asm volatile("s_mov_b32 m0, %0\n\ts_nop 0\n\tbuffer_load_dwordx4 %1, %2, %3 offen lds" :: "s"(m0v), "v"(voff), "s"(srd), "s"(so) : "memory");
}
DI void touch4(unsigned m0v, unsigned voff, u4 srd, unsigned so) {
  asm volatile(NOPS "s_mov_b32 m0, %0\n\ts_nop 0\n\tbuffer_load_dword %1, %2, %3 offen lds" :: "s"(rfl(m0v)), "v"(voff), "s"(rfl(srd)), "s"(rfl(so)) : "memory");
}
#define SB() __builtin_amdgcn_sched_barrier(0)
template <int N> DI void bar_v() { SB(); asm volatile("s_waitcnt vmcnt(%0) lgkmcnt(0)\n\ts_barrier" ::"i"(N) : "memory"); SB(); }
DI void bar_l() { SB(); asm volatile("s_waitcnt lgkmcnt(0)\n\ts_barrier" ::: "memory"); SB(); }

DI bf8 ld128(LAS char* sm, unsigned a) { return __builtin_bit_cast(bf8, *(LAS s8*)(sm + a)); }
DI bf8 ldtr(LAS char* sm, unsigned a0, unsigned a1) {
  s4 x = __builtin_amdgcn_ds_read_tr16_b64_v4i16((LAS s4*)(sm + a0)), y = __builtin_amdgcn_ds_read_tr16_b64_v4i16((LAS s4*)(sm + a1));
  return __builtin_bit_cast(bf8, __builtin_shufflevector(x, y, 0, 1, 2, 3, 4, 5, 6, 7));
}
DI unsigned opq(unsigned v) { asm volatile("" : "+v"(v)); return v; }
DI bf8 undef8() { bf8 v; asm volatile("" : "=v"(v)); return v; }
DI unsigned sel(bool c, unsigned a, unsigned b) { return c ? a : b; }
DI int opqs(int v) { v = __builtin_amdgcn_readfirstlane(v); asm volatile("" : "+s"(v)); return v; }
DI int lane_id() { unsigned z = 0; asm volatile("" : "+v"(z)); return __builtin_amdgcn_mbcnt_hi(~0u, __builtin_amdgcn_mbcnt_lo(~0u, z)); }
DI unsigned pk(float a, float b) { f2 x = {a, b}; return __builtin_bit_cast(unsigned, __builtin_convertvector(x, bf2)); }
DI f4 mfma16(bf8 a, bf8 b, f4 c) { return __builtin_amdgcn_mfma_f32_16x16x32_bf16(a, b, c, 0, 0, 0); }
DI f16v mfma32(bf8 a, bf8 b, f16v c) { return __builtin_amdgcn_mfma_f32_32x32x16_bf16(a, b, c, 0, 0, 0); }

constexpr u64 w_table() { u64 t = 0; for (int c = 0; c < 24; ++c) { const int n = c < 12 ? 1 + (c & 1) : (c == 13 || c == 16 || c == 19 || c == 21 || c == 23) ? 1 : (c == 14 || c == 17 || c == 20 || c == 22) ? 2 : 0; t |= (u64)n << (2 * c); } return t; }
#define WHOLE ((1u<<19)|(1u<<21))

__global__ __launch_bounds__(512, 2) void fused_kernel(const char* __restrict__ x, const char* __restrict__ K, const char* __restrict__ V,
                                                       const char* __restrict__ wq, const char* __restrict__ wo, char* ws, char* __restrict__ out
) {
  __shared__ __attribute__((aligned(16))) char smem[LDS_BYTES];
  LAS char* sm = (LAS char*)smem;
  const int wave = __builtin_amdgcn_readfirstlane(threadIdx.x >> 6), w = wave & 3;
  const int xcd = blockIdx.x & 7, idx = blockIdx.x >> 3, b = idx >> 1, hp = idx & 1, h0 = xcd * 4 + hp * 2, mem = xcd * 2 + hp;
  const u4 sK = mk_srd(K + (size_t)(b * 32 + h0) * ITEM, 2 * ITEM), sV = mk_srd(V + (size_t)(b * 32 + h0) * ITEM, 2 * ITEM);
  const u4 sQ = mk_srd(wq + h0 * 384, 640 * 12288 - h0 * 384), sO = mk_srd(wo + h0 * (192 * 1280), 384 * 1280);
  char* const part = ws + PART_OFF;
  GAS u64* const sync = (GAS u64*)ws;
  const unsigned w4k = w * 4096u, OOB = 0x7ffffff0u;
  auto v_k = [&](int l) { return (unsigned)((32 * w + (l >> 3)) * 384 + (((l & 7) ^ (l >> 4)) << 4)); };

  auto reduce_slice = [&](int i) {
    const int r = lane_id(), pi = 5 * mem + i, qh = pi / 40, L = (pi % 40) >> 2, o = pi & 3, un = r >> 4, h = (r >> 3) & 1, q8 = r & 7;
    const int nblk = 2 * L + (un >> 1), hf = un & 1;
    const auto rs = __builtin_amdgcn_make_buffer_rsrc(part + (size_t)b * 16 * 81920, 0, 16 * 81920, 0x00020000);
    const unsigned vo = ((qh * 20 + nblk) * 2 + hf) * 1024 + (32 * h + 8 * o + q8) * 16;
    u4 v[16];
    sfor<0, 16>([&](auto p_) { constexpr int p = decltype(p_)::v; v[p] = __builtin_amdgcn_raw_buffer_load_b128(rs, vo, p * 81920, 16); });
    float s[8] = {};
    sfor<0, 16>([&](auto p_) { constexpr int p = decltype(p_)::v; sfor<0, 4>([&](auto e_) { constexpr int e = decltype(e_)::v;
      s[2 * e] += __uint_as_float(v[p][e] << 16); s[2 * e + 1] += __uint_as_float(v[p][e] & 0xffff0000u); }); });
    char* dst = out + (size_t)(b * 64 + 32 * qh + 8 * o + q8) * 1280 + (32 * nblk + 16 * hf + 4 * h) * 2;
    *(u2*)dst = u2{pk(s[0], s[1]), pk(s[2], s[3])};
    *(u2*)(dst + 16) = u2{pk(s[4], s[5]), pk(s[6], s[7])}; };

  if (wave < 4) {
    __builtin_amdgcn_s_setprio(ATT_PRIO);
    const int lane = lane_id();
    STAMP_DECL; STAMP(0);
    const int r16 = lane & 15, g = lane >> 4, q4 = (lane & 15) >> 2, p = lane & 3;
    unsigned w_so = 0, w_g = 0, w_v = (32 * w + (lane >> 3)) * 12288 + (((lane & 7) ^ (((lane >> 4) & 1) << 2)) << 4), w_st = 8 * 12288, w_dT = 128 * 12288, w_dG = 128u - 5u * 128 * 12288;
    u4 w_srd = sQ;
    auto w_tile = [&](unsigned lb) { dma4<false>(lb + w4k, w_v, w_v, w_srd, w_so, w_st); w_so += w_dT; };
    auto w_pair = [&]() { w_tile(XB); w_tile(XB + US); };
    auto w_triple = [&]() { w_tile(YB); w_tile(YB + US); w_tile(YB + 2 * US); w_so += w_dG;
      if (++w_g == 6) { const int l = lane_id(); w_so = 0; w_v = (16 * w + (l >> 4)) * 1280 + (((l & 15) ^ ((l >> 4) << 2)) << 4); w_st = 4 * 1280; w_dT = 256; w_dG = 64 * 1280 - 5 * 256; w_srd = sO; } };
    auto w_issue = [&](int c) { const unsigned n = (unsigned)(w_table() >> (2 * c + 2)) & 3; if (n & 1) w_pair(); if (n & 2) w_triple(); };
    const u64 epoch = __hip_atomic_load(sync, __ATOMIC_RELAXED, __HIP_MEMORY_SCOPE_AGENT);
    { const unsigned trow = (b * 40 + w * 10) * 12288;
      bar_l();
      w_pair();
      STAMP(104);
      bar_l();
      STAMP(105);
      w_triple();
      bar_v<12>();
      STAMP(106);
      touch4(w4k, lane < 20 ? trow + (lane >> 1) * 12288 + (lane & 1) * 128 : OOB, sQ, 128);
      { const unsigned vK = v_k(lane); for (int u = 0; u < 2; ++u) dma4<true>(u * US + w4k, vK, vK ^ 64, sK, u * 49152, 3072); }
      STAMP(107);
      bar_v<9>();
      bar_l();
      w_pair();
      bar_v<12>();
      STAMP(1); }
    const unsigned ka0 = r16 * 128 + ((g ^ ((r16 >> 1) & 7)) << 4);
    const unsigned va0 = (4 * g + q4) * 128 + p * 8 + (((2 * g + (q4 >> 1)) & 3) << 5);
    const unsigned wa0 = (16 * w + r16) * 128 + (((g >> 1) ^ ((r16 >> 1) & 7)) << 4) + (g & 1) * 8 + HB;
    bf8 qc[2], qn[3][2], pf[16]; f4 S[32], O[4], Ls; u2 hA[4]; float m0, mc[2], alpha, linv;
    const u4 ones_u = {0x3f803f80u, 0x3f803f80u, 0x3f803f80u, 0x3f803f80u};
    auto read_q = [&](bf8* q, int hs) { const unsigned a = opq(ka0 + HB + w * 2048 + hs * 8192); q[0] = ld128(sm, a); q[1] = ld128(sm, a ^ 64); };
    auto k_unit = [&](auto kb_, auto un_, auto first_) { constexpr int kb = decltype(kb_)::v, un = decltype(un_)::v, PD = 8; constexpr bool first = decltype(first_)::v;
      const unsigned a0 = opq(ka0) + kb * KVS + un * US, a1 = a0 ^ 64;
      bf8 a[16];
      auto rd = [&](auto i_) { constexpr int i = decltype(i_)::v; a[i] = ld128(sm, ((i & 1) ? a1 : a0) + (i >> 1) * 2048); };
      sfor<0, PD>(rd); SB();
      sfor<0, 8>([&](auto j_) { constexpr int j = decltype(j_)::v, blk = 16 * kb + 8 * un + j;
        f4 c; if constexpr (first) c = f4{0, 0, 0, 0}; else c = S[blk];
        c = mfma16(a[2 * j + 1], qc[1], c); S[blk] = mfma16(a[2 * j], qc[0], c);
        if constexpr (2 * j + PD < 16) { rd(IC<2 * j + PD>{}); rd(IC<2 * j + PD + 1>{}); }
        SB(); }); };
    auto max_half = [&](auto h_) { constexpr int h = decltype(h_)::v;
      float m = fmaxf(S[16 * h][0], S[16 * h][1]), m2 = fmaxf(S[16 * h][2], S[16 * h][3]);
      sfor<1, 16>([&](auto b_) { constexpr int i = 16 * h + decltype(b_)::v; m = fmaxf(fmaxf(m, S[i][0]), S[i][1]); m2 = fmaxf(fmaxf(m2, S[i][2]), S[i][3]); });
      m = fmaxf(m, m2);
      { auto r = __builtin_amdgcn_permlane16_swap(__float_as_uint(m), __float_as_uint(m), false, false); m = fmaxf(__uint_as_float(r[0]), __uint_as_float(r[1])); }
      { auto r = __builtin_amdgcn_permlane32_swap(__float_as_uint(m), __float_as_uint(m), false, false); m = fmaxf(__uint_as_float(r[0]), __uint_as_float(r[1])); }
      if constexpr (h == 0) m0 = m; else { m = fmaxf(m, m0); alpha = __builtin_amdgcn_exp2f((m0 - m) * SC); }
      mc[h] = -m * SC; };
    auto exp_kk = [&](auto kk_) { constexpr int kk = decltype(kk_)::v; float e[8];
      sfor<0, 8>([&](auto j_) { constexpr int j = decltype(j_)::v; e[j] = __builtin_amdgcn_exp2f(fmaf(S[2 * kk + (j >> 2)][j & 3], SC, mc[kk >> 3])); });
      u4 u = {pk(e[0], e[1]), pk(e[2], e[3]), pk(e[4], e[5]), pk(e[6], e[7])}; pf[kk] = __builtin_bit_cast(bf8, u); };
    auto v_unit = [&](auto kb_, auto un_, auto ls_) { constexpr int kb = decltype(kb_)::v, un = decltype(un_)::v, PD = 6; constexpr bool ls = decltype(ls_)::v;
      const unsigned a0 = opq(va0) + kb * KVS + un * US;
      bf8 a[16];
      auto rd = [&](auto i_) { constexpr int i = decltype(i_)::v; const unsigned ad = (a0 ^ ((i & 3) << 5)) + (i >> 2) * 4096; a[i] = ldtr(sm, ad, ad + 2048); };
      sfor<0, PD>(rd);
      if constexpr (kb == 1 && un == 0) { sfor<0, 4>([&](auto d_) { constexpr int db = decltype(d_)::v; O[db] *= alpha; }); if constexpr (ls) Ls *= alpha; }
      SB();
      sfor<0, 8>([&](auto j_) { constexpr int j = decltype(j_)::v, kk = 8 * kb + 4 * un + (j >> 1), d0 = (j & 1) * 2;
        f4 c0, c1; if constexpr (kk == 0) { c0 = f4{0, 0, 0, 0}; c1 = c0; } else { c0 = O[d0]; c1 = O[d0 + 1]; }
        O[d0 + 1] = mfma16(a[2 * j + 1], pf[kk], c1); O[d0] = mfma16(a[2 * j], pf[kk], c0);
        if constexpr (ls && (j & 1)) { f4 cl; if constexpr (kk == 0) cl = f4{0, 0, 0, 0}; else cl = Ls; Ls = mfma16(__builtin_bit_cast(bf8, ones_u), pf[kk], cl); }
        if constexpr (2 * j + PD < 16) { rd(IC<2 * j + PD>{}); rd(IC<2 * j + PD + 1>{}); }
        SB(); }); };
    auto finish_o = [&]() { sfor<0, 4>([&](auto d_) { constexpr int db = decltype(d_)::v;
        hA[db] = u2{pk(O[db][0] * linv, O[db][1] * linv), pk(O[db][2] * linv, O[db][3] * linv)}; }); };
    auto write_o = [&](int hs) { const unsigned a = opq(wa0 + hs * 8192);
      sfor<0, 4>([&](auto d_) { constexpr int db = decltype(d_)::v; *(LAS u2*)(sm + (a ^ (db << 5))) = hA[db]; }); };
    sfor<0, 3>([&](auto i_) { qn[decltype(i_)::v][0] = bf8{}; qn[decltype(i_)::v][1] = bf8{}; });

    auto bar_a = [&](int c, bool second) { if ((((unsigned)WHOLE >> c) & 1) == (unsigned)second) { STAMP(6 + 4 * c); bar_v<0>(); } };
    const u64 tag = epoch + 1;
    auto w_and_c = [&](int c) { STAMP(5 + 4 * c);
      if (c == GATE_C && w == 0 && lane_id() == 0) __hip_atomic_store(sync + 512 + b * 16 + mem, tag, __ATOMIC_RELAXED, __HIP_MEMORY_SCOPE_AGENT);
      w_issue(c); STAMP(7 + 4 * c); bar_l(); };
#pragma clang loop unroll(disable)
    for (int it = 0; it < 2; ++it) {
      it = opqs(it);
      const int c0 = 12 * it;
      sfor<0, 6>([&](auto r_) { constexpr int r = decltype(r_)::v, ch = r >> 1, kb = r & 1;
        if constexpr (kb == 0) { bf8 n[2] = {qn[ch][0], qn[ch][1]}; if (it == 0) read_q(n, ch & 1); qc[0] = n[0]; qc[1] = n[1]; }
        k_unit(IC<kb>{}, IC<0>{}, IC<ch == 0>{});
        if constexpr (r == 5) sfor<1, 4>(exp_kk);
        bar_a(c0 + r, false);
        k_unit(IC<kb>{}, IC<1>{}, IC<ch == 0>{});
        bar_a(c0 + r, true);
        if constexpr (r == 4) { max_half(IC<0>{}); sfor<0, 1>(exp_kk); }
        if constexpr (r == 5) { sfor<4, 5>(exp_kk); max_half(IC<1>{}); }
        w_and_c(c0 + r); });
      if (it == 0) read_q(qn[0], 1);
      v_unit(IC<0>{}, IC<0>{}, IC<1>{}); sfor<5, 9>(exp_kk);
      bar_a(c0 + 6, false);
      v_unit(IC<0>{}, IC<1>{}, IC<1>{});
      bar_a(c0 + 6, true);
      sfor<9, 12>(exp_kk);
      w_and_c(c0 + 6);
      v_unit(IC<1>{}, IC<0>{}, IC<1>{}); sfor<12, 16>(exp_kk);
      bar_a(c0 + 7, false);
      v_unit(IC<1>{}, IC<1>{}, IC<1>{}); linv = 1.0f / Ls[0]; finish_o();
      if (it == 1) write_o(1);
      bar_a(c0 + 7, true);
      w_and_c(c0 + 7);
#pragma clang loop unroll(disable)
      for (int ch = 1; ch < 3; ++ch) {
        ch = opqs(ch);
        const int c = c0 + 6 + 2 * ch;
        if (it == 0) { bf8 n[2]; read_q(n, (ch + 1) & 1); qn[1][0] = qn[2][0]; qn[1][1] = qn[2][1]; qn[2][0] = n[0]; qn[2][1] = n[1]; }
        v_unit(IC<0>{}, IC<0>{}, IC<0>{});
        bar_a(c, false);
        v_unit(IC<0>{}, IC<1>{}, IC<0>{});
        bar_a(c, true);
        if (it == 0) write_o((ch - 1) & 1);
        w_and_c(c);
        if (c == 22) w_triple();
        v_unit(IC<1>{}, IC<0>{}, IC<0>{});
        if (c == 22) bar_l(); else bar_a(c + 1, false);
        v_unit(IC<1>{}, IC<1>{}, IC<0>{}); finish_o();
        if (it == 1 || ch == 2) write_o((it + ch) & 1);
        if (c == 22) break;
        bar_a(c + 1, true);
        w_and_c(c + 1);
      }
    }
    bar_v<12>();
    bar_v<0>();
    if (w == 0) {
      const int l = lane_id(); bool ok = false;
      for (unsigned spins = 0; spins < (1u << 20) && !ok; ++spins) { bool e = true;
        sfor<0, 4>([&](auto j_) { e &= __hip_atomic_load(sync + 512 + 64 * decltype(j_)::v + l, __ATOMIC_RELAXED, __HIP_MEMORY_SCOPE_AGENT) == tag; });
        ok = __all(e); if (!ok) __builtin_amdgcn_s_sleep(2); }
      if (ok && l == 0 && blockIdx.x == 0) __hip_atomic_store(sync, tag, __ATOMIC_RELAXED, __HIP_MEMORY_SCOPE_AGENT);
    }
    bar_l();
    bar_l();
    if (w == 0) {
      const int l = lane_id();
      if (l == 0) __hip_atomic_store(sync + 1024 + b * 16 + mem, tag, __ATOMIC_RELAXED, __HIP_MEMORY_SCOPE_AGENT);
      for (unsigned spins = 0; spins < (1u << 20); ++spins) {
        u64 f = tag; if (l < 16) f = __hip_atomic_load(sync + 1024 + b * 16 + l, __ATOMIC_RELAXED, __HIP_MEMORY_SCOPE_AGENT);
        if (__all(f == tag)) break;
        __builtin_amdgcn_s_sleep(1); }
      __builtin_amdgcn_fence(__ATOMIC_ACQUIRE, "wavefront");
    }
    bar_l();
    reduce_slice(w);
    STAMP_FLUSH();
  } else {
    __builtin_amdgcn_s_setprio(PROJ_PRIO);
    const int lane = lane_id();
    STAMP_DECL; STAMP(0);
    { const u4 sX = mk_srd(x + b * 81920, 81920);
      const unsigned vX = (16 * w + (lane >> 4)) * 1280 + (((lane & 15) ^ (lane >> 4)) << 4);
      const unsigned t0 = (hp ? HB : 0) + w4k;
      touch4(t0, lane < 10 ? (b * 40 + w * 10 + lane) * 12288 : OOB, sQ, 0);
      { u64 pc; asm volatile("s_getpc_b64 %0" : "=s"(pc));
        const u4 sC = {(unsigned)pc, (unsigned)(pc >> 32) & 0xffffu, CODE_BYTES, 0x00020000u};
        const u4 st = {w == 0 ? sK[0] : w == 1 ? sV[0] : w == 2 ? sO[0] : sC[0], w == 0 ? sK[1] : w == 1 ? sV[1] : w == 2 ? sO[1] : sC[1], w == 3 ? sC[2] : sK[2], sK[3]};
        touch4(t0, w == 3 ? (lane < 8 ? (idx * 8 + lane) * 128 : OOB) : lane < 2 ? lane * ITEM : OOB, st, 0); }
      for (int t = 0; t < 5; ++t) { const unsigned tt = hp ? 4 - t : t, lb = tt == 4 ? HB : tt * US;
        sfor<0, 4>([&](auto j_) { constexpr int j = decltype(j_)::v; dma1(lb + w4k + j * 1024, vX ^ (j << 6), sX, tt * 256 + j * 4 * 1280); }); }
      bar_l(); }
    const int qh = w & 1, ah = w >> 1, h = lane >> 5, b16 = (lane >> 4) & 1, q4 = (lane & 15) >> 2, p = lane & 3;
    const unsigned row = 32 * qh + (lane & 31), f8 = (row >> 1) & 7;
    const unsigned qa0 = (8 * h + q4) * 128 + ((ah ^ (q4 >> 1)) << 6) + 32 * b16 + 8 * p;
    const unsigned oa0 = (8 * h + q4) * 256 + (((2 * ah) ^ q4) << 6) + 32 * b16 + 8 * p;
    const unsigned ha0 = row * 128 + ((h ^ f8) << 4) + HB;
    constexpr int PD = 6;
    const unsigned vK = v_k(lane), vV = (32 * w + (lane >> 3)) * 384 + (((lane & 7) ^ (((lane >> 4) & 3) << 1)) << 4);
    unsigned k_so = 2 * 49152, k_n = 1, k_vE = vK, k_vO = vK ^ 64; u4 k_srd = sK;
    auto kv_tile = [&]() { const unsigned lb = (k_n & 1) * KVS + w4k;
      dma4<true>(lb, k_vE, k_vO, k_srd, k_so, 3072); dma4<true>(lb + US, k_vE, k_vO, k_srd, k_so + 49152, 3072);
      ++k_n; k_so += (k_n & 1) ? 2 * 49152u : 128u - 2 * 49152u;
      if (k_n == 6 || k_n == 12 || k_n == 18) { const bool isV = k_n != 12;
        k_so = k_n == 6 ? 0 : ITEM; k_srd[0] = sel(isV, sV[0], sK[0]); k_srd[1] = sel(isV, sV[1], sK[1]); k_vE = sel(isV, vV, vK); k_vO = sel(isV, vV, vK ^ 64); } };
    auto seam = [&](bool kv) { STAMP(k_n >= 2 ? 4 * k_n - 3 : 2);
      if (((unsigned)WHOLE >> (k_n - 1)) & 1) bar_v<0>(); else bar_v<4>();
      STAMP(k_n >= 2 ? 4 * k_n - 2 : 1);
      if (kv) {
        if (k_n == 2 || k_n == 7 || k_n == 15) { const unsigned l = lane_id(), tl = b * 120 + w * 30 + l; const bool q = k_n == 2;
          const u4 st = {sel(q, sQ[0], sO[0]), sel(q, sQ[1], sO[1]), sel(q, sQ[2], sO[2]), sQ[3]};
          touch4((k_n & 1) * KVS + w4k, l < 30 ? sel(q, (tl / 3) * 12288 + (tl % 3) * 128, tl * 128) : OOB, st, q ? 384 : k_n == 7 ? 0 : 245760); }
        kv_tile(); STAMP(k_n >= 3 ? 4 * k_n - 5 : 2); bar_v<8>(); } else bar_v<0>();
      STAMPA(k_n >= 2 ? 4 * k_n - 4 : 2); };

#pragma clang loop unroll(disable)
    for (int pass = DRY ? 0 : 1; pass < 2; ++pass) {
    pass = opqs(pass);
    const bool dry = pass == 0;
    {
      bf8 xf[40]; f16v Qa = {};
      sfor<0, 40>([&](auto s_) { xf[decltype(s_)::v] = undef8(); });
      auto q_run = [&](auto t0_, auto n_, unsigned lb) { constexpr int T0 = decltype(t0_)::v, N = 8 * decltype(n_)::v;
        const unsigned a0 = opq(qa0 + lb);
        bf8 a[N];
        auto rd = [&](auto i_) { constexpr int i = decltype(i_)::v; a[i] = ldtr(sm, a0 + i * 2048, a0 + i * 2048 + 512); };
        sfor<0, PD>(rd); SB();
        sfor<0, N / 2>([&](auto j_) { constexpr int i = 2 * decltype(j_)::v;
          f16v c; if constexpr (T0 == 0 && i == 0) c = f16v{}; else c = Qa;
          c = mfma32(a[i + 1], xf[8 * T0 + i + 1], c); Qa = mfma32(a[i], xf[8 * T0 + i], c);
          if constexpr (i + PD < N) { rd(IC<i + PD>{}); rd(IC<i + PD + 1>{}); }
          SB(); }); };
      if (!dry) { bar_v<0>();
        STAMP(105);
        const unsigned xa = row * 256 + ((h ^ (row & 15)) << 4);
        sfor<0, 40>([&](auto s_) { constexpr int s = decltype(s_)::v; xf[s] = ld128(sm, (xa ^ ((s & 7) << 5)) + (s >= 32 ? HB : (s >> 3) * US)); });
        bar_l();
        STAMP(106); }
#pragma clang loop unroll(disable)
      for (int gq = 0; gq < 6; ++gq) {
        gq = opqs(gq);
        q_run(IC<0>{}, IC<2>{}, XB);
        if (!dry) seam(gq > 0);
        q_run(IC<2>{}, IC<3>{}, YB);
        if (dry) break;
        { const unsigned a = opq((ha0 ^ (h << 4)) + (gq & 1) * 8192 + 8 * h) ^ (ah << 6);
          sfor<0, 4>([&](auto i_) { constexpr int i = decltype(i_)::v;
            *(LAS u2*)(sm + (a ^ (i << 4))) = u2{pk(Qa[4 * i], Qa[4 * i + 1]), pk(Qa[4 * i + 2], Qa[4 * i + 3])}; }); }
        seam(true);
      }
    }
    {
      f16v acc[10]; bf8 ob[4];
      sfor<0, 10>([&](auto i_) { acc[decltype(i_)::v] = f16v{}; });
      const auto rsP = __builtin_amdgcn_make_buffer_rsrc(part + (size_t)(b * 16 + mem) * 81920 + (qh * 40 + 4 * ah) * 1024, 0, dry ? 0 : 81920, 0x00020000);
      auto store_t = [&](auto t_) { constexpr int T = decltype(t_)::v; const unsigned pv = lane_id() * 16;
        sfor<0, 4>([&](auto i_) { constexpr int j = decltype(i_)::v >> 1, hf = decltype(i_)::v & 1; const f16v& a = acc[2 * T + j];
          u4 u = {pk(a[8 * hf], a[8 * hf + 1]), pk(a[8 * hf + 2], a[8 * hf + 3]), pk(a[8 * hf + 4], a[8 * hf + 5]), pk(a[8 * hf + 6], a[8 * hf + 7])};
          __builtin_amdgcn_raw_buffer_store_b128(u, rsP, pv, ((4 * T + j) * 2 + hf) * 1024, PART_AUX); }); };
      auto o_run = [&](auto t0_, auto n_, unsigned lb, bool last) { constexpr int T0 = decltype(t0_)::v, N = 8 * decltype(n_)::v;
        const unsigned a0 = opq(oa0 + lb), a1 = a0 ^ 64;
        bf8 a[N];
        auto rd = [&](auto i_) { constexpr int i = decltype(i_)::v; const unsigned ad = ((i & 4) ? a1 : a0) + (i >> 3) * US + (i & 3) * 4096; a[i] = ldtr(sm, ad, ad + 1024); };
        sfor<0, PD>(rd); SB();
        sfor<0, N / 2>([&](auto j_) { constexpr int j = decltype(j_)::v, i = 2 * j, blk = 2 * T0 + (j >> 1), k = (j & 1) * 2;
          f16v c = mfma32(a[i + 1], ob[k + 1], acc[blk]); acc[blk] = mfma32(a[i], ob[k], c);
          if constexpr (i + PD < N) { rd(IC<i + PD>{}); rd(IC<i + PD + 1>{}); }
          SB();
          }); };
#pragma clang loop unroll(disable)
      for (int go = dry ? 5 : 0; go < 6; ++go) {
        go = opqs(go);
        const bool last = go == 5;
        { const unsigned ad = opq(ha0 + (go & 1) * 8192); sfor<0, 4>([&](auto k_) { constexpr int k = decltype(k_)::v; ob[k] = ld128(sm, ad ^ (k << 5)); }); }
        o_run(IC<0>{}, IC<2>{}, XB, last);
        if (!last) seam(true); else if (!dry) bar_l();
        o_run(IC<2>{}, IC<3>{}, YB, last);
        if (last) break;
        seam(go != 4);
        if (go < 3) seam(true);
        if (go == 4) { STAMP(100); bar_l(); STAMP(101); }
      }
      STAMP(120);
      bar_l();
      STAMP(121);
      sfor<0, 5>(store_t);
    }
    }
    STAMP(102);
    bar_v<0>();
    STAMP(103);
    bar_l();
    STAMP(124);
    if (w == 0) reduce_slice(4);
    STAMP_FLUSH();
  }
}

extern "C" void kernel_launch(void* const* d_in, const int* in_sizes, int n_in, void* d_out, int out_size, void* d_ws, size_t ws_size, hipStream_t stream) {
  fused_kernel<<<256, 512, 0, stream>>>((const char*)d_in[0], (const char*)d_in[1], (const char*)d_in[2], (const char*)d_in[3], (const char*)d_in[4], (char*)d_ws, (char*)d_out
  );
}
```

```cpp
#include <hip/hip_runtime.h>
#include <hip/hip_bf16.h>
#include <stdint.h>

typedef short s4 __attribute__((ext_vector_type(4)));
typedef short s8 __attribute__((ext_vector_type(8)));
typedef __bf16 bf8 __attribute__((ext_vector_type(8)));
typedef __bf16 bf2 __attribute__((ext_vector_type(2)));
typedef float f2 __attribute__((ext_vector_type(2)));
typedef float f4 __attribute__((ext_vector_type(4)));
typedef float f16v __attribute__((ext_vector_type(16)));
typedef unsigned u2 __attribute__((ext_vector_type(2)));
typedef unsigned u4 __attribute__((ext_vector_type(4)));
typedef unsigned long long u64;
#define LAS __attribute__((address_space(3)))
#define GAS __attribute__((address_space(1)))
#define DI __device__ __forceinline__

template <int N> struct IC { static constexpr int v = N; };
template <int A, int B, class F> DI void sfor(F&& f) { if constexpr (A < B) { f(IC<A>{}); sfor<A + 1, B>(f); } }

constexpr unsigned US = 16384, KVS = 32768, XB = 65536, YB = 98304, HB = 147456, LDS_BYTES = 163840;
constexpr unsigned PART_OFF = 1 << 20;
constexpr unsigned ITEM = 196608;
constexpr float SC = 0.07216878364870322f * 1.4426950408889634f;

#define PROJ_PRIO 0
#define ATT_PRIO 1
#define PIN 1
#define TRI_PRIO 0
#define OWN 0
#define INTERLEAVE 1
#define POLL_SLEEP 2
#define GATE_SLEEP 2
#define SPREAD 0
#define TQ 2
#define TO0 2
#define TO1 2
#define PTOUCH 0
#define RD_AUX 16
#define PHASE_D 65
#define PHASE_N 2
#define PKFMA 0
#define GATE_C 21
#define CODE_BYTES 30000u
#define DRY 0
#define PART_AUX 16
#define STAMP(i) do {} while (0)
#define STAMPA(i) do {} while (0)
#define STAMP_DECL
#define STAMP_FLUSH() do {} while (0)

#define NOPS
template <class T> DI T rfl(T v) { return v; }
DI u4 mk_srd(const void* p, unsigned bytes) {
  const u64 a = (u64)p;
  u4 r = {(unsigned)__builtin_amdgcn_readfirstlane((unsigned)a), (unsigned)__builtin_amdgcn_readfirstlane((unsigned)(a >> 32) & 0xffffu), bytes, 0x00020000u};
  return r;
}
template <bool NT> DI void dma4(unsigned mb, unsigned v0, unsigned v1, u4 srd, unsigned so, unsigned st) {
  unsigned t;
#define DMA4_BODY(POL) \
  asm volatile(NOPS "s_mov_b32 m0, %1\n\ts_add_u32 %0, %5, %6\n\tbuffer_load_dwordx4 %2, %4, %5 offen " POL "lds\n\t" \
               "s_add_u32 m0, %1, 0x400\n\ts_nop 0\n\tbuffer_load_dwordx4 %3, %4, %0 offen " POL "lds\n\t" \
               "s_add_u32 m0, %1, 0x800\n\ts_add_u32 %0, %0, %6\n\tbuffer_load_dwordx4 %2, %4, %0 offen " POL "lds\n\t" \
               "s_add_u32 m0, %1, 0xc00\n\ts_add_u32 %0, %0, %6\n\tbuffer_load_dwordx4 %3, %4, %0 offen " POL "lds" \
               : "=&s"(t) : "s"(rfl(mb)), "v"(v0), "v"(v1), "s"(rfl(srd)), "s"(rfl(so)), "s"(rfl(st)) : "memory", "scc")
  if constexpr (NT) DMA4_BODY("nt "); else DMA4_BODY("");
#undef DMA4_BODY
}
template <int J> DI void dma1nt(unsigned mb, unsigned voff, u4 srd, unsigned so) {
  unsigned t;
  asm volatile(NOPS "s_add_u32 m0, %1, %2\n\ts_add_u32 %0, %3, %4\n\tbuffer_load_dwordx4 %5, %6, %0 offen nt lds"
               : "=&s"(t) : "s"(rfl(mb)), "i"(J * 1024), "s"(rfl(so)), "i"(J * 3072), "v"(voff), "s"(rfl(srd)) : "memory", "scc");
}
DI void dma1(unsigned m0v, unsigned voff, u4 srd, unsigned so) {
  asm volatile("s_mov_b32 m0, %0\n\ts_nop 0\n\tbuffer_load_dwordx4 %1, %2, %3 offen lds" :: "s"(m0v), "v"(voff), "s"(srd), "s"(so) : "memory");
}
DI void touch4(unsigned m0v, unsigned voff, u4 srd, unsigned so) {
  asm volatile(NOPS "s_mov_b32 m0, %0\n\ts_nop 0\n\tbuffer_load_dword %1, %2, %3 offen lds" :: "s"(rfl(m0v)), "v"(voff), "s"(rfl(srd)), "s"(rfl(so)) : "memory");
}
#define SB() __builtin_amdgcn_sched_barrier(0)
template <int N> DI void bar_v() { SB(); asm volatile("s_waitcnt vmcnt(%0) lgkmcnt(0)\n\ts_barrier" ::"i"(N) : "memory"); SB(); }
DI void bar_l() { SB(); asm volatile("s_waitcnt lgkmcnt(0)\n\ts_barrier" ::: "memory"); SB(); }

DI bf8 ld128(LAS char* sm, unsigned a) { return __builtin_bit_cast(bf8, *(LAS s8*)(sm + a)); }
DI bf8 ldtr(LAS char* sm, unsigned a0, unsigned a1) {
  s4 x = __builtin_amdgcn_ds_read_tr16_b64_v4i16((LAS s4*)(sm + a0)), y = __builtin_amdgcn_ds_read_tr16_b64_v4i16((LAS s4*)(sm + a1));
  return __builtin_bit_cast(bf8, __builtin_shufflevector(x, y, 0, 1, 2, 3, 4, 5, 6, 7));
}
DI unsigned opq(unsigned v) { asm volatile("" : "+v"(v)); return v; }
DI bf8 undef8() { bf8 v; asm volatile("" : "=v"(v)); return v; }
DI unsigned sel(bool c, unsigned a, unsigned b) { return c ? a : b; }
DI int opqs(int v) { v = __builtin_amdgcn_readfirstlane(v); asm volatile("" : "+s"(v)); return v; }
DI int lane_id() { unsigned z = 0; asm volatile("" : "+v"(z)); return __builtin_amdgcn_mbcnt_hi(~0u, __builtin_amdgcn_mbcnt_lo(~0u, z)); }
DI unsigned pk(float a, float b) { f2 x = {a, b}; return __builtin_bit_cast(unsigned, __builtin_convertvector(x, bf2)); }
DI f4 mfma16(bf8 a, bf8 b, f4 c) { return __builtin_amdgcn_mfma_f32_16x16x32_bf16(a, b, c, 0, 0, 0); }
DI f16v mfma32(bf8 a, bf8 b, f16v c) { return __builtin_amdgcn_mfma_f32_32x32x16_bf16(a, b, c, 0, 0, 0); }

constexpr u64 w_table() { u64 t = 0; for (int c = 0; c < 24; ++c) { const int n = c < 12 ? 1 + (c & 1) : (c == 13 || c == 16 || c == 19 || c == 21 || c == 23) ? 1 : (c == 14 || c == 17 || c == 20 || c == 22) ? 2 : 0; t |= (u64)n << (2 * c); } return t; }
#define WHOLE ((1u<<19)|(1u<<21))

__global__ __launch_bounds__(512, 2) void fused_kernel(const char* __restrict__ x, const char* __restrict__ K, const char* __restrict__ V,
                                                       const char* __restrict__ wq, const char* __restrict__ wo, char* ws, char* __restrict__ out
) {
  __shared__ __attribute__((aligned(16))) char smem[LDS_BYTES];
  LAS char* sm = (LAS char*)smem;
  const int wave = __builtin_amdgcn_readfirstlane(threadIdx.x >> 6), w = wave & 3;
  const int xcd = blockIdx.x & 7, idx = blockIdx.x >> 3, b = idx >> 1, hp = idx & 1, h0 = xcd * 4 + hp * 2, mem = xcd * 2 + hp;
  const u4 sK = mk_srd(K + (size_t)(b * 32 + h0) * ITEM, 2 * ITEM), sV = mk_srd(V + (size_t)(b * 32 + h0) * ITEM, 2 * ITEM);
  const u4 sQ = mk_srd(wq + h0 * 384, 640 * 12288 - h0 * 384), sO = mk_srd(wo + h0 * (192 * 1280), 384 * 1280);
  char* const part = ws + PART_OFF;
  GAS u64* const sync = (GAS u64*)(ws + b * 1024);
  const unsigned w4k = w * 4096u, OOB = 0x7ffffff0u;
  auto v_k = [&](int l) { return (unsigned)((32 * w + (l >> 3)) * 384 + (((l & 7) ^ (l >> 4)) << 4)); };

  auto reduce_slice = [&](int i, u64 tag) {
    const int r = lane_id(), pi = 5 * mem + i, qh = pi / 40, L = (pi % 40) >> 2, o = pi & 3, un = r >> 4, h = (r >> 3) & 1, q8 = r & 7;
    const int nblk = 2 * L + (un >> 1), hf = un & 1;
    const auto rs = __builtin_amdgcn_make_buffer_rsrc(part + (size_t)b * 16 * 81920, 0, 16 * 81920, 0x00020000);
    const unsigned vo = ((qh * 20 + nblk) * 2 + hf) * 1024 + (32 * h + 8 * o + q8) * 16;
    u4 v[16];
    sfor<0, 16>([&](auto p_) { constexpr int p = decltype(p_)::v; v[p] = __builtin_amdgcn_raw_buffer_load_b128(rs, (OWN && p == mem) ? OOB : vo, p * 81920, RD_AUX); });
    if (OWN) { const u4 own = *(LAS u4*)(sm + i * 1024 + r * 16); sfor<0, 16>([&](auto p_) { constexpr int p = decltype(p_)::v; if (p == mem) v[p] = own; }); }
    float s[8] = {};
    sfor<0, 16>([&](auto p_) { constexpr int p = decltype(p_)::v; sfor<0, 4>([&](auto e_) { constexpr int e = decltype(e_)::v;
      s[2 * e] += __uint_as_float(v[p][e] << 16); s[2 * e + 1] += __uint_as_float(v[p][e] & 0xffff0000u); }); });
    const u2 lo = {pk(s[0], s[1]), pk(s[2], s[3])}, hi = {pk(s[4], s[5]), pk(s[6], s[7])};
    const unsigned s0 = h ? lo[0] : hi[0], s1 = h ? lo[1] : hi[1];
    const unsigned r0 = __builtin_amdgcn_update_dpp(0u, s0, 0x128, 0xf, 0xf, false), r1 = __builtin_amdgcn_update_dpp(0u, s1, 0x128, 0xf, 0xf, false);
    *(u4*)(out + (size_t)(b * 64 + 32 * qh + 8 * o + q8) * 1280 + (32 * nblk + 16 * hf + 8 * h) * 2) = h ? u4{r0, r1, hi[0], hi[1]} : u4{lo[0], lo[1], r0, r1}; };
  const u64 epoch = __hip_atomic_load(sync, __ATOMIC_RELAXED, __HIP_MEMORY_SCOPE_AGENT);

  if (wave < 4) {
    __builtin_amdgcn_s_setprio(ATT_PRIO);
    const int lane = lane_id();
    STAMP_DECL; STAMP(0);
    const int r16 = lane & 15, g = lane >> 4, q4 = (lane & 15) >> 2, p = lane & 3;
    unsigned w_so = 0, w_g = 0, w_v = (32 * w + (lane >> 3)) * 12288 + (((lane & 7) ^ (((lane >> 4) & 1) << 2)) << 4), w_st = 8 * 12288, w_dT = 128 * 12288, w_dG = 128u - 5u * 128 * 12288;
    u4 w_srd = sQ;
    auto w_tile = [&](unsigned lb) { dma4<false>(lb + w4k, w_v, w_v, w_srd, w_so, w_st); w_so += w_dT; };
    auto w_pair = [&]() { w_tile(XB); w_tile(XB + US); };
    auto w_triple = [&]() { w_tile(YB); w_tile(YB + US); w_tile(YB + 2 * US); w_so += w_dG;
      if (++w_g == 6) { const int l = lane_id(); w_so = 0; w_v = (16 * w + (l >> 4)) * 1280 + (((l & 15) ^ ((l >> 4) << 2)) << 4); w_st = 4 * 1280; w_dT = 256; w_dG = 64 * 1280 - 5 * 256; w_srd = sO; } };
    auto w_issue = [&](int c) { const unsigned n = (unsigned)(w_table() >> (2 * c + 2)) & 3; if (n & 1) w_pair(); if (n & 2) w_triple(); };
    { const unsigned trow = (b * 40 + w * 10) * 12288;
      bar_l();
      w_pair();
      STAMP(104);
      bar_l();
      STAMP(105);
      w_triple();
      bar_v<12>();
      STAMP(106);
      touch4(w4k, lane < 20 ? trow + (lane >> 1) * 12288 + (lane & 1) * 128 : OOB, sQ, 128);
      { const unsigned tl = b * 120 + w * 30 + lane;
        if (PTOUCH & 1) touch4(w4k, lane < 30 ? (tl / 3) * 12288 + (tl % 3) * 128 : OOB, sQ, 384);
        if (PTOUCH & 2) touch4(w4k, lane < 30 ? tl * 128 : OOB, sO, 0);
        if (PTOUCH & 4) touch4(w4k, lane < 30 ? tl * 128 : OOB, sO, 245760); }
      { const unsigned vK = v_k(lane); for (int u = 0; u < 2; ++u) dma4<true>(u * US + w4k, vK, vK ^ 64, sK, u * 49152, 3072); }
      STAMP(107);
      bar_v<9 + (PTOUCH & 1) + ((PTOUCH >> 1) & 1) + ((PTOUCH >> 2) & 1)>();
      bar_l();
      w_pair();
      if (PHASE_D) { const unsigned ph = PHASE_N == 2 ? (b & 1) : PHASE_N == 3 ? (b % 3) : PHASE_N == 5 ? (unsigned)hp : PHASE_N == 8 ? (b & 7) : (b & 3); const u64 t0_ = __builtin_amdgcn_s_memrealtime(); while (__builtin_amdgcn_s_memrealtime() - t0_ < ph * PHASE_D) __builtin_amdgcn_s_sleep(1); }
      bar_v<12>();
      STAMP(1); }
    const unsigned ka0 = r16 * 128 + ((g ^ ((r16 >> 1) & 7)) << 4);
    const unsigned va0 = (4 * g + q4) * 128 + p * 8 + (((2 * g + (q4 >> 1)) & 3) << 5);
    const unsigned wa0 = (16 * w + r16) * 128 + (((g >> 1) ^ ((r16 >> 1) & 7)) << 4) + (g & 1) * 8 + HB;
    bf8 qc[2], qn[3][2], pf[16]; f4 S[32], O[4], Ls; u2 hA[4]; float m0, mc[2], alpha, linv;
    const u4 ones_u = {0x3f803f80u, 0x3f803f80u, 0x3f803f80u, 0x3f803f80u};
    auto read_q = [&](bf8* q, int hs) { const unsigned a = opq(ka0 + HB + w * 2048 + hs * 8192); q[0] = ld128(sm, a); q[1] = ld128(sm, a ^ 64); };
    auto k_unit = [&](auto kb_, auto un_, auto first_) { constexpr int kb = decltype(kb_)::v, un = decltype(un_)::v, PD = 8; constexpr bool first = decltype(first_)::v;
      const unsigned a0 = opq(ka0) + kb * KVS + un * US, a1 = a0 ^ 64;
      bf8 a[16];
      auto rd = [&](auto i_) { constexpr int i = decltype(i_)::v; a[i] = ld128(sm, ((i & 1) ? a1 : a0) + (i >> 1) * 2048); };
      sfor<0, PD>(rd); SB();
      sfor<0, 8>([&](auto j_) { constexpr int j = decltype(j_)::v, blk = 16 * kb + 8 * un + j;
        f4 c; if constexpr (first) c = f4{0, 0, 0, 0}; else c = S[blk];
        c = mfma16(a[2 * j + 1], qc[1], c); S[blk] = mfma16(a[2 * j], qc[0], c);
        if constexpr (2 * j + PD < 16) { rd(IC<2 * j + PD>{}); rd(IC<2 * j + PD + 1>{}); }
        SB(); }); };
    auto max_half = [&](auto h_) { constexpr int h = decltype(h_)::v;
      float m = fmaxf(S[16 * h][0], S[16 * h][1]), m2 = fmaxf(S[16 * h][2], S[16 * h][3]);
      sfor<1, 16>([&](auto b_) { constexpr int i = 16 * h + decltype(b_)::v; m = fmaxf(fmaxf(m, S[i][0]), S[i][1]); m2 = fmaxf(fmaxf(m2, S[i][2]), S[i][3]); });
      m = fmaxf(m, m2);
      { auto r = __builtin_amdgcn_permlane16_swap(__float_as_uint(m), __float_as_uint(m), false, false); m = fmaxf(__uint_as_float(r[0]), __uint_as_float(r[1])); }
      { auto r = __builtin_amdgcn_permlane32_swap(__float_as_uint(m), __float_as_uint(m), false, false); m = fmaxf(__uint_as_float(r[0]), __uint_as_float(r[1])); }
      if constexpr (h == 0) m0 = m; else { m = fmaxf(m, m0); alpha = __builtin_amdgcn_exp2f((m0 - m) * SC); }
      mc[h] = -m * SC; };
    auto exp_kk = [&](auto kk_) { constexpr int kk = decltype(kk_)::v; float e[8];
      sfor<0, 8>([&](auto j_) { constexpr int j = decltype(j_)::v; e[j] = __builtin_amdgcn_exp2f(fmaf(S[2 * kk + (j >> 2)][j & 3], SC, mc[kk >> 3])); });
      u4 u = {pk(e[0], e[1]), pk(e[2], e[3]), pk(e[4], e[5]), pk(e[6], e[7])}; if (PIN) asm volatile("" : "+v"(u)); pf[kk] = __builtin_bit_cast(bf8, u); };
    auto v_unit = [&](auto kb_, auto un_, auto ls_) { constexpr int kb = decltype(kb_)::v, un = decltype(un_)::v, PD = 6; constexpr bool ls = decltype(ls_)::v;
      const unsigned a0 = opq(va0) + kb * KVS + un * US;
      bf8 a[16];
      auto rd = [&](auto i_) { constexpr int i = decltype(i_)::v; const unsigned ad = (a0 ^ ((i & 3) << 5)) + (i >> 2) * 4096; a[i] = ldtr(sm, ad, ad + 2048); };
      sfor<0, PD>(rd);
      if constexpr (kb == 1 && un == 0) { sfor<0, 4>([&](auto d_) { constexpr int db = decltype(d_)::v; O[db] *= alpha; }); if constexpr (ls) Ls *= alpha; }
      SB();
      sfor<0, 8>([&](auto j_) { constexpr int j = decltype(j_)::v, kk = 8 * kb + 4 * un + (j >> 1), d0 = (j & 1) * 2;
        f4 c0, c1; if constexpr (kk == 0) { c0 = f4{0, 0, 0, 0}; c1 = c0; } else { c0 = O[d0]; c1 = O[d0 + 1]; }
        O[d0 + 1] = mfma16(a[2 * j + 1], pf[kk], c1); O[d0] = mfma16(a[2 * j], pf[kk], c0);
        if constexpr (ls && (j & 1)) { f4 cl; if constexpr (kk == 0) cl = f4{0, 0, 0, 0}; else cl = Ls; Ls = mfma16(__builtin_bit_cast(bf8, ones_u), pf[kk], cl); }
        if constexpr (2 * j + PD < 16) { rd(IC<2 * j + PD>{}); rd(IC<2 * j + PD + 1>{}); }
        SB(); }); };
    auto finish_o = [&]() { sfor<0, 4>([&](auto d_) { constexpr int db = decltype(d_)::v;
        u2 t_ = u2{pk(O[db][0] * linv, O[db][1] * linv), pk(O[db][2] * linv, O[db][3] * linv)}; if (PIN) asm volatile("" : "+v"(t_)); hA[db] = t_; }); };
    auto write_o = [&](int hs) { const unsigned a = opq(wa0 + hs * 8192);
      sfor<0, 4>([&](auto d_) { constexpr int db = decltype(d_)::v; *(LAS u2*)(sm + (a ^ (db << 5))) = hA[db]; }); };
    sfor<0, 3>([&](auto i_) { qn[decltype(i_)::v][0] = bf8{}; qn[decltype(i_)::v][1] = bf8{}; });

    auto bar_a = [&](int c, bool second) { if ((((unsigned)WHOLE >> c) & 1) == (unsigned)second) { STAMP(6 + 4 * c); bar_v<0>(); } };
    const u64 tag = epoch + 1;
    auto w_and_c = [&](int c) { STAMP(5 + 4 * c);
      w_issue(c); STAMP(7 + 4 * c); bar_l(); };
#pragma clang loop unroll(disable)
    for (int it = 0; it < 2; ++it) {
      it = opqs(it);
      const int c0 = 12 * it;
      sfor<0, 6>([&](auto r_) { constexpr int r = decltype(r_)::v, ch = r >> 1, kb = r & 1;
        if constexpr (kb == 0) { bf8 n[2] = {qn[ch][0], qn[ch][1]}; if (it == 0) read_q(n, ch & 1); qc[0] = n[0]; qc[1] = n[1]; }
        k_unit(IC<kb>{}, IC<0>{}, IC<ch == 0>{});
        if constexpr (r == 5) sfor<1, 4>(exp_kk);
        bar_a(c0 + r, false);
        k_unit(IC<kb>{}, IC<1>{}, IC<ch == 0>{});
        bar_a(c0 + r, true);
        if constexpr (r == 4) { max_half(IC<0>{}); sfor<0, 1>(exp_kk); }
        if constexpr (r == 5) { sfor<4, 5>(exp_kk); max_half(IC<1>{}); }
        w_and_c(c0 + r); });
      if (it == 0) read_q(qn[0], 1);
      v_unit(IC<0>{}, IC<0>{}, IC<1>{}); sfor<5, 9>(exp_kk);
      bar_a(c0 + 6, false);
      v_unit(IC<0>{}, IC<1>{}, IC<1>{});
      bar_a(c0 + 6, true);
      sfor<9, 12>(exp_kk);
      w_and_c(c0 + 6);
      v_unit(IC<1>{}, IC<0>{}, IC<1>{}); sfor<12, 16>(exp_kk);
      bar_a(c0 + 7, false);
      v_unit(IC<1>{}, IC<1>{}, IC<1>{}); linv = 1.0f / Ls[0]; finish_o();
      if (it == 1) write_o(1);
      bar_a(c0 + 7, true);
      w_and_c(c0 + 7);
#pragma clang loop unroll(disable)
      for (int ch = 1; ch < 3; ++ch) {
        ch = opqs(ch);
        const int c = c0 + 6 + 2 * ch;
        if (it == 0) { bf8 n[2]; read_q(n, (ch + 1) & 1); qn[1][0] = qn[2][0]; qn[1][1] = qn[2][1]; qn[2][0] = n[0]; qn[2][1] = n[1]; }
        v_unit(IC<0>{}, IC<0>{}, IC<0>{});
        bar_a(c, false);
        v_unit(IC<0>{}, IC<1>{}, IC<0>{});
        bar_a(c, true);
        if (it == 0) write_o((ch - 1) & 1);
        w_and_c(c);
        if (c == 22) w_triple();
        v_unit(IC<1>{}, IC<0>{}, IC<0>{});
        if (c == 22) bar_l(); else bar_a(c + 1, false);
        v_unit(IC<1>{}, IC<1>{}, IC<0>{}); finish_o();
        if (it == 1 || ch == 2) write_o((it + ch) & 1);
        if (c == 22) break;
        bar_a(c + 1, true);
        w_and_c(c + 1);
      }
    }
    bar_v<12>();
    bar_v<0>();
    bar_l();
    if (w == 0) {
      const int l = lane_id(); bool ok = false;
      if (l == 0) __hip_atomic_store(sync + 32 + mem, tag, __ATOMIC_RELAXED, __HIP_MEMORY_SCOPE_AGENT);
      for (unsigned spins = 0; spins < (1u << 20) && !ok; ++spins) {
        u64 f = tag; if (l < 16) f = __hip_atomic_load(sync + 32 + l, __ATOMIC_RELAXED, __HIP_MEMORY_SCOPE_AGENT);
        ok = __all(f == tag); if (!ok) __builtin_amdgcn_s_sleep(1); }
      if (ok && l == 0 && mem == 0) __hip_atomic_store(sync, tag, __ATOMIC_RELAXED, __HIP_MEMORY_SCOPE_AGENT);
      __builtin_amdgcn_fence(__ATOMIC_ACQUIRE, "wavefront");
    }
    bar_l();
    reduce_slice(w, tag);
    STAMP_FLUSH();
  } else {
    __builtin_amdgcn_s_setprio(PROJ_PRIO);
    const int lane = lane_id();
    STAMP_DECL; STAMP(0);
    { const u4 sX = mk_srd(x + b * 81920, 81920);
      const unsigned vX = (16 * w + (lane >> 4)) * 1280 + (((lane & 15) ^ (lane >> 4)) << 4);
      const unsigned t0 = (hp ? HB : 0) + w4k;
      touch4(t0, lane < 10 ? (b * 40 + w * 10 + lane) * 12288 : OOB, sQ, 0);
      { u64 pc; asm volatile("s_getpc_b64 %0" : "=s"(pc));
        const u4 sC = {(unsigned)pc, (unsigned)(pc >> 32) & 0xffffu, CODE_BYTES, 0x00020000u};
        const u4 st = {w == 0 ? sK[0] : w == 1 ? sV[0] : w == 2 ? sO[0] : sC[0], w == 0 ? sK[1] : w == 1 ? sV[1] : w == 2 ? sO[1] : sC[1], w == 3 ? sC[2] : sK[2], sK[3]};
        touch4(t0, w == 3 ? (lane < 8 ? (idx * 8 + lane) * 128 : OOB) : lane < 2 ? lane * ITEM : OOB, st, 0); }
      for (int t = 0; t < 5; ++t) { const unsigned tt = hp ? 4 - t : t, lb = tt == 4 ? HB : tt * US;
        sfor<0, 4>([&](auto j_) { constexpr int j = decltype(j_)::v; dma1(lb + w4k + j * 1024, vX ^ (j << 6), sX, tt * 256 + j * 4 * 1280); }); }
      bar_l(); }
    const int qh = w & 1, ah = w >> 1, h = lane >> 5, b16 = (lane >> 4) & 1, q4 = (lane & 15) >> 2, p = lane & 3;
    const unsigned row = 32 * qh + (lane & 31), f8 = (row >> 1) & 7;
    const unsigned qa0 = (8 * h + q4) * 128 + ((ah ^ (q4 >> 1)) << 6) + 32 * b16 + 8 * p;
    const unsigned oa0 = (8 * h + q4) * 256 + (((2 * ah) ^ q4) << 6) + 32 * b16 + 8 * p;
    const unsigned ha0 = row * 128 + ((h ^ f8) << 4) + HB;
    constexpr int PD = 6;
    const unsigned vK = v_k(lane), vV = (32 * w + (lane >> 3)) * 384 + (((lane & 7) ^ (((lane >> 4) & 3) << 1)) << 4);
    unsigned k_so = 2 * 49152, k_n = 1, k_vE = vK, k_vO = vK ^ 64; u4 k_srd = sK;
    auto kv_tile = [&]() { const unsigned lb = (k_n & 1) * KVS + w4k;
      dma4<true>(lb, k_vE, k_vO, k_srd, k_so, 3072); dma4<true>(lb + US, k_vE, k_vO, k_srd, k_so + 49152, 3072);
      ++k_n; k_so += (k_n & 1) ? 2 * 49152u : 128u - 2 * 49152u;
      if (k_n == 6 || k_n == 12 || k_n == 18) { const bool isV = k_n != 12;
        k_so = k_n == 6 ? 0 : ITEM; k_srd[0] = sel(isV, sV[0], sK[0]); k_srd[1] = sel(isV, sV[1], sK[1]); k_vE = sel(isV, vV, vK); k_vO = sel(isV, vV, vK ^ 64); } };
    auto seam = [&](bool kv) { STAMP(k_n >= 2 ? 4 * k_n - 3 : 2);
      if (((unsigned)WHOLE >> (k_n - 1)) & 1) bar_v<0>(); else bar_v<4>();
      STAMP(k_n >= 2 ? 4 * k_n - 2 : 1);
      if (kv) {
        if (k_n == TQ || k_n == TO0 || k_n == TO1) { const unsigned l = lane_id(), tl = b * 120 + w * 30 + l, ld = (k_n & 1) * KVS + w4k;
          if (k_n == TQ) touch4(ld, l < 30 ? (tl / 3) * 12288 + (tl % 3) * 128 : OOB, sQ, 384);
          if (k_n == TO0) touch4(ld, l < 30 ? tl * 128 : OOB, sO, 0);
          if (k_n == TO1) touch4(ld, l < 30 ? tl * 128 : OOB, sO, 245760); }
        kv_tile(); STAMP(k_n >= 3 ? 4 * k_n - 5 : 2); bar_v<8>(); } else bar_v<0>();
      STAMPA(k_n >= 2 ? 4 * k_n - 4 : 2); };

#pragma clang loop unroll(disable)
    for (int pass = DRY ? 0 : 1; pass < 2; ++pass) {
    pass = opqs(pass);
    const bool dry = pass == 0;
    {
      bf8 xf[40]; f16v Qa = {};
      sfor<0, 40>([&](auto s_) { xf[decltype(s_)::v] = undef8(); });
      auto q_run = [&](auto t0_, auto n_, unsigned lb) { constexpr int T0 = decltype(t0_)::v, N = 8 * decltype(n_)::v;
        const unsigned a0 = opq(qa0 + lb);
        bf8 a[N];
        auto rd = [&](auto i_) { constexpr int i = decltype(i_)::v; a[i] = ldtr(sm, a0 + i * 2048, a0 + i * 2048 + 512); };
        sfor<0, PD>(rd); SB();
        sfor<0, N / 2>([&](auto j_) { constexpr int i = 2 * decltype(j_)::v;
          f16v c; if constexpr (T0 == 0 && i == 0) c = f16v{}; else c = Qa;
          c = mfma32(a[i + 1], xf[8 * T0 + i + 1], c); Qa = mfma32(a[i], xf[8 * T0 + i], c);
          if constexpr (i + PD < N) { rd(IC<i + PD>{}); rd(IC<i + PD + 1>{}); }
          SB(); }); };
      if (!dry) { bar_v<0>();
        STAMP(105);
        const unsigned xa = row * 256 + ((h ^ (row & 15)) << 4);
        sfor<0, 40>([&](auto s_) { constexpr int s = decltype(s_)::v; xf[s] = ld128(sm, (xa ^ ((s & 7) << 5)) + (s >= 32 ? HB : (s >> 3) * US)); });
        bar_l();
        STAMP(106); }
#pragma clang loop unroll(disable)
      for (int gq = 0; gq < 6; ++gq) {
        gq = opqs(gq);
        q_run(IC<0>{}, IC<2>{}, XB);
        if (!dry) seam(gq > 0);
        if (TRI_PRIO & 1) __builtin_amdgcn_s_setprio(2);
        q_run(IC<2>{}, IC<3>{}, YB);
        if (TRI_PRIO & 1) __builtin_amdgcn_s_setprio(0);
        if (dry) break;
        { const unsigned a = opq((ha0 ^ (h << 4)) + (gq & 1) * 8192 + 8 * h) ^ (ah << 6);
          sfor<0, 4>([&](auto i_) { constexpr int i = decltype(i_)::v;
            *(LAS u2*)(sm + (a ^ (i << 4))) = u2{pk(Qa[4 * i], Qa[4 * i + 1]), pk(Qa[4 * i + 2], Qa[4 * i + 3])}; }); }
        seam(true);
      }
    }
    {
      f16v acc[10]; bf8 ob[4];
      sfor<0, 10>([&](auto i_) { acc[decltype(i_)::v] = f16v{}; });
      const auto rsP = __builtin_amdgcn_make_buffer_rsrc(part + (size_t)(b * 16 + mem) * 81920 + (qh * 40 + 4 * ah) * 1024, 0, dry ? 0 : 81920, 0x00020000);
      auto store_t = [&](auto t_) { constexpr int T = decltype(t_)::v; const unsigned l = lane_id(), pi = 40 * qh + 8 * T + 4 * ah + ((l & 31) >> 3) - 5 * mem; const bool own = OWN && pi < 5;
        sfor<0, 4>([&](auto i_) { constexpr int j = decltype(i_)::v >> 1, hf = decltype(i_)::v & 1; const f16v& a = acc[2 * T + j];
          u4 u = {pk(a[8 * hf], a[8 * hf + 1]), pk(a[8 * hf + 2], a[8 * hf + 3]), pk(a[8 * hf + 4], a[8 * hf + 5]), pk(a[8 * hf + 6], a[8 * hf + 7])};
          __builtin_amdgcn_raw_buffer_store_b128(u, rsP, own ? OOB : l * 16, ((4 * T + j) * 2 + hf) * 1024, PART_AUX);
          if (own) *(LAS u4*)(sm + pi * 1024 + (2 * j + hf) * 256 + (l >> 5) * 128 + (l & 7) * 16) = u; }); };
      auto o_run = [&](auto t0_, auto n_, unsigned lb, bool last) { constexpr int T0 = decltype(t0_)::v, N = 8 * decltype(n_)::v;
        const unsigned a0 = opq(oa0 + lb), a1 = a0 ^ 64;
        bf8 a[N];
        auto rd = [&](auto i_) { constexpr int i = decltype(i_)::v; const unsigned ad = ((i & 4) ? a1 : a0) + (i >> 3) * US + (i & 3) * 4096; a[i] = ldtr(sm, ad, ad + 1024); };
        sfor<0, PD>(rd); SB();
        sfor<0, N / 2>([&](auto j_) { constexpr int j = decltype(j_)::v, i = 2 * j, blk = 2 * T0 + (j >> 1), k = (j & 1) * 2;
          f16v c = mfma32(a[i + 1], ob[k + 1], acc[blk]); acc[blk] = mfma32(a[i], ob[k], c);
          if constexpr (i + PD < N) { rd(IC<i + PD>{}); rd(IC<i + PD + 1>{}); }
          SB();
          if constexpr ((j & 3) == 3) { if (last) store_t(IC<T0 + (j >> 2)>{}); }
          }); };
#pragma clang loop unroll(disable)
      for (int go = dry ? 5 : 0; go < 6; ++go) {
        go = opqs(go);
        const bool last = go == 5;
        { const unsigned ad = opq(ha0 + (go & 1) * 8192); sfor<0, 4>([&](auto k_) { constexpr int k = decltype(k_)::v; ob[k] = ld128(sm, ad ^ (k << 5)); }); }
        o_run(IC<0>{}, IC<2>{}, XB, last);
        if (!last) seam(true); else if (!dry) bar_l();
        if (TRI_PRIO & 2) __builtin_amdgcn_s_setprio(2);
        o_run(IC<2>{}, IC<3>{}, YB, last);
        if (TRI_PRIO & 2) __builtin_amdgcn_s_setprio(0);
        if (last) break;
        seam(go != 4);
        if (go < 3) seam(true);
        if (go == 4) { STAMP(100); bar_l(); STAMP(101); }
      }
    }
    }
    STAMP(102);
    bar_v<0>();
    STAMP(103);
    bar_l();
    STAMP(124);
    if (w == 0) reduce_slice(4, epoch + 1);
    STAMP_FLUSH();
  }
}

extern "C" void kernel_launch(void* const* d_in, const int* in_sizes, int n_in, void* d_out, int out_size, void* d_ws, size_t ws_size, hipStream_t stream) {
  fused_kernel<<<256, 512, 0, stream>>>((const char*)d_in[0], (const char*)d_in[1], (const char*)d_in[2], (const char*)d_in[3], (const char*)d_in[4], (char*)d_ws, (char*)d_out
  );
}
```

```cpp
#include <hip/hip_runtime.h>
#include <hip/hip_bf16.h>
#include <stdint.h>

typedef short s4 __attribute__((ext_vector_type(4)));
typedef short s8 __attribute__((ext_vector_type(8)));
typedef __bf16 bf8 __attribute__((ext_vector_type(8)));
typedef __bf16 bf2 __attribute__((ext_vector_type(2)));
typedef float f2 __attribute__((ext_vector_type(2)));
typedef float f4 __attribute__((ext_vector_type(4)));
typedef float f16v __attribute__((ext_vector_type(16)));
typedef unsigned u2 __attribute__((ext_vector_type(2)));
typedef unsigned u4 __attribute__((ext_vector_type(4)));
typedef unsigned long long u64;
#define LAS __attribute__((address_space(3)))
#define GAS __attribute__((address_space(1)))
#define DI __device__ __forceinline__

template <int N> struct IC { static constexpr int v = N; };
template <int A, int B, class F> DI void sfor(F&& f) { if constexpr (A < B) { f(IC<A>{}); sfor<A + 1, B>(f); } }

constexpr unsigned US = 16384, KVS = 32768, XB = 65536, YB = 98304, HB = 147456, LDS_BYTES = 163840;
constexpr unsigned REG = 16 * 81920 + 8192;
constexpr unsigned ITEM = 196608;
constexpr float SC = 0.07216878364870322f * 1.4426950408889634f;

#define PROJ_PRIO 0
#define ATT_PRIO 1
#define WARM 1
#define PIN 1
#define TRI_PRIO 0
#define OWN 0
#define INTERLEAVE 1
#define POLL_SLEEP 2
#define GATE_SLEEP 2
#define SPREAD 0
#define TQ 2
#define TO0 2
#define TO1 2
#define PTOUCH 0
#define RD_AUX 16
#define PHASE_D 65
#define PHASE_N 2
#define PKFMA 0
#define GATE_C 21
#define CODE_BYTES 30000u
#define DRY 0
#define PART_AUX 16
#define STAMP(i) do {} while (0)
#define STAMPA(i) do {} while (0)
#define STAMP_DECL
#define STAMP_FLUSH() do {} while (0)

#define NOPS
template <class T> DI T rfl(T v) { return v; }
DI u4 mk_srd(const void* p, unsigned bytes) {
  const u64 a = (u64)p;
  u4 r = {(unsigned)__builtin_amdgcn_readfirstlane((unsigned)a), (unsigned)__builtin_amdgcn_readfirstlane((unsigned)(a >> 32) & 0xffffu), bytes, 0x00020000u};
  return r;
}
template <bool NT> DI void dma4(unsigned mb, unsigned v0, unsigned v1, u4 srd, unsigned so, unsigned st) {
  unsigned t;
#define DMA4_BODY(POL) \
  asm volatile(NOPS "s_mov_b32 m0, %1\n\ts_add_u32 %0, %5, %6\n\tbuffer_load_dwordx4 %2, %4, %5 offen " POL "lds\n\t" \
               "s_add_u32 m0, %1, 0x400\n\ts_nop 0\n\tbuffer_load_dwordx4 %3, %4, %0 offen " POL "lds\n\t" \
               "s_add_u32 m0, %1, 0x800\n\ts_add_u32 %0, %0, %6\n\tbuffer_load_dwordx4 %2, %4, %0 offen " POL "lds\n\t" \
               "s_add_u32 m0, %1, 0xc00\n\ts_add_u32 %0, %0, %6\n\tbuffer_load_dwordx4 %3, %4, %0 offen " POL "lds" \
               : "=&s"(t) : "s"(rfl(mb)), "v"(v0), "v"(v1), "s"(rfl(srd)), "s"(rfl(so)), "s"(rfl(st)) : "memory", "scc")
  if constexpr (NT) DMA4_BODY("nt "); else DMA4_BODY("");
#undef DMA4_BODY
}
template <int J> DI void dma1nt(unsigned mb, unsigned voff, u4 srd, unsigned so) {
  unsigned t;
  asm volatile(NOPS "s_add_u32 m0, %1, %2\n\ts_add_u32 %0, %3, %4\n\tbuffer_load_dwordx4 %5, %6, %0 offen nt lds"
               : "=&s"(t) : "s"(rfl(mb)), "i"(J * 1024), "s"(rfl(so)), "i"(J * 3072), "v"(voff), "s"(rfl(srd)) : "memory", "scc");
}
DI void dma1(unsigned m0v, unsigned voff, u4 srd, unsigned so) {
  asm volatile("s_mov_b32 m0, %0\n\ts_nop 0\n\tbuffer_load_dwordx4 %1, %2, %3 offen lds" :: "s"(m0v), "v"(voff), "s"(srd), "s"(so) : "memory");
}
DI void touch4(unsigned m0v, unsigned voff, u4 srd, unsigned so) {
  asm volatile(NOPS "s_mov_b32 m0, %0\n\ts_nop 0\n\tbuffer_load_dword %1, %2, %3 offen lds" :: "s"(rfl(m0v)), "v"(voff), "s"(rfl(srd)), "s"(rfl(so)) : "memory");
}
#define SB() __builtin_amdgcn_sched_barrier(0)
template <int N> DI void bar_v() { SB(); asm volatile("s_waitcnt vmcnt(%0) lgkmcnt(0)\n\ts_barrier" ::"i"(N) : "memory"); SB(); }
DI void bar_l() { SB(); asm volatile("s_waitcnt lgkmcnt(0)\n\ts_barrier" ::: "memory"); SB(); }

DI bf8 ld128(LAS char* sm, unsigned a) { return __builtin_bit_cast(bf8, *(LAS s8*)(sm + a)); }
DI bf8 ldtr(LAS char* sm, unsigned a0, unsigned a1) {
  s4 x = __builtin_amdgcn_ds_read_tr16_b64_v4i16((LAS s4*)(sm + a0)), y = __builtin_amdgcn_ds_read_tr16_b64_v4i16((LAS s4*)(sm + a1));
  return __builtin_bit_cast(bf8, __builtin_shufflevector(x, y, 0, 1, 2, 3, 4, 5, 6, 7));
}
DI unsigned opq(unsigned v) { asm volatile("" : "+v"(v)); return v; }
DI bf8 undef8() { bf8 v; asm volatile("" : "=v"(v)); return v; }
DI unsigned sel(bool c, unsigned a, unsigned b) { return c ? a : b; }
DI int opqs(int v) { v = __builtin_amdgcn_readfirstlane(v); asm volatile("" : "+s"(v)); return v; }
DI int lane_id() { unsigned z = 0; asm volatile("" : "+v"(z)); return __builtin_amdgcn_mbcnt_hi(~0u, __builtin_amdgcn_mbcnt_lo(~0u, z)); }
DI unsigned pk(float a, float b) { f2 x = {a, b}; return __builtin_bit_cast(unsigned, __builtin_convertvector(x, bf2)); }
DI f4 mfma16(bf8 a, bf8 b, f4 c) { return __builtin_amdgcn_mfma_f32_16x16x32_bf16(a, b, c, 0, 0, 0); }
DI f16v mfma32(bf8 a, bf8 b, f16v c) { return __builtin_amdgcn_mfma_f32_32x32x16_bf16(a, b, c, 0, 0, 0); }

constexpr u64 w_table() { u64 t = 0; for (int c = 0; c < 24; ++c) { const int n = c < 12 ? 1 + (c & 1) : (c == 13 || c == 16 || c == 19 || c == 21 || c == 23) ? 1 : (c == 14 || c == 17 || c == 20 || c == 22) ? 2 : 0; t |= (u64)n << (2 * c); } return t; }
#define WHOLE ((1u<<19)|(1u<<21))

__global__ __launch_bounds__(512, 2) void fused_kernel(const char* __restrict__ x, const char* __restrict__ K, const char* __restrict__ V,
                                                       const char* __restrict__ wq, const char* __restrict__ wo, char* ws, char* __restrict__ out
) {
  __shared__ __attribute__((aligned(16))) char smem[LDS_BYTES];
  LAS char* sm = (LAS char*)smem;
  const int wave = __builtin_amdgcn_readfirstlane(threadIdx.x >> 6), w = wave & 3;
  const int xcd = blockIdx.x & 7, idx = blockIdx.x >> 3, b = idx >> 1, hp = idx & 1, h0 = xcd * 4 + hp * 2, mem = xcd * 2 + hp;
  const u4 sK = mk_srd(K + (size_t)(b * 32 + h0) * ITEM, 2 * ITEM), sV = mk_srd(V + (size_t)(b * 32 + h0) * ITEM, 2 * ITEM);
  const u4 sQ = mk_srd(wq + h0 * 384, 640 * 12288 - h0 * 384), sO = mk_srd(wo + h0 * (192 * 1280), 384 * 1280);
  char* const part = ws + (size_t)b * REG + 4096;
  GAS u64* const sync = (GAS u64*)(ws + (size_t)b * REG);
  const unsigned w4k = w * 4096u, OOB = 0x7ffffff0u;
  auto v_k = [&](int l) { return (unsigned)((32 * w + (l >> 3)) * 384 + (((l & 7) ^ (l >> 4)) << 4)); };

  auto reduce_slice = [&](int i, u64 tag) {
    const int r = lane_id(), pi = 5 * mem + i, qh = pi / 40, L = (pi % 40) >> 2, o = pi & 3, un = r >> 4, h = (r >> 3) & 1, q8 = r & 7;
    const int nblk = 2 * L + (un >> 1), hf = un & 1;
    const auto rs = __builtin_amdgcn_make_buffer_rsrc(part, 0, 16 * 81920, 0x00020000);
    const unsigned vo = ((qh * 20 + nblk) * 2 + hf) * 1024 + (32 * h + 8 * o + q8) * 16;
    u4 v[16];
    sfor<0, 16>([&](auto p_) { constexpr int p = decltype(p_)::v; v[p] = __builtin_amdgcn_raw_buffer_load_b128(rs, (OWN && p == mem) ? OOB : vo, p * 81920, RD_AUX); });
    if (OWN) { const u4 own = *(LAS u4*)(sm + i * 1024 + r * 16); sfor<0, 16>([&](auto p_) { constexpr int p = decltype(p_)::v; if (p == mem) v[p] = own; }); }
    float s[8] = {};
    sfor<0, 16>([&](auto p_) { constexpr int p = decltype(p_)::v; sfor<0, 4>([&](auto e_) { constexpr int e = decltype(e_)::v;
      s[2 * e] += __uint_as_float(v[p][e] << 16); s[2 * e + 1] += __uint_as_float(v[p][e] & 0xffff0000u); }); });
    const u2 lo = {pk(s[0], s[1]), pk(s[2], s[3])}, hi = {pk(s[4], s[5]), pk(s[6], s[7])};
    const unsigned s0 = h ? lo[0] : hi[0], s1 = h ? lo[1] : hi[1];
    const unsigned r0 = __builtin_amdgcn_update_dpp(0u, s0, 0x128, 0xf, 0xf, false), r1 = __builtin_amdgcn_update_dpp(0u, s1, 0x128, 0xf, 0xf, false);
    *(u4*)(out + (size_t)(b * 64 + 32 * qh + 8 * o + q8) * 1280 + (32 * nblk + 16 * hf + 8 * h) * 2) = h ? u4{r0, r1, hi[0], hi[1]} : u4{lo[0], lo[1], r0, r1}; };
  const u64 epoch = __hip_atomic_load(sync, __ATOMIC_RELAXED, __HIP_MEMORY_SCOPE_AGENT);

  if (wave < 4) {
    __builtin_amdgcn_s_setprio(ATT_PRIO);
    const int lane = lane_id();
    STAMP_DECL; STAMP(0);
    const int r16 = lane & 15, g = lane >> 4, q4 = (lane & 15) >> 2, p = lane & 3;
    unsigned w_so = 0, w_g = 0, w_v = (32 * w + (lane >> 3)) * 12288 + (((lane & 7) ^ (((lane >> 4) & 1) << 2)) << 4), w_st = 8 * 12288, w_dT = 128 * 12288, w_dG = 128u - 5u * 128 * 12288;
    u4 w_srd = sQ;
    auto w_tile = [&](unsigned lb) { dma4<false>(lb + w4k, w_v, w_v, w_srd, w_so, w_st); w_so += w_dT; };
    auto w_pair = [&]() { w_tile(XB); w_tile(XB + US); };
    auto w_triple = [&]() { w_tile(YB); w_tile(YB + US); w_tile(YB + 2 * US); w_so += w_dG;
      if (++w_g == 6) { const int l = lane_id(); w_so = 0; w_v = (16 * w + (l >> 4)) * 1280 + (((l & 15) ^ ((l >> 4) << 2)) << 4); w_st = 4 * 1280; w_dT = 256; w_dG = 64 * 1280 - 5 * 256; w_srd = sO; } };
    auto w_issue = [&](int c) { const unsigned n = (unsigned)(w_table() >> (2 * c + 2)) & 3; if (n & 1) w_pair(); if (n & 2) w_triple(); };
    { const unsigned trow = (b * 40 + w * 10) * 12288;
      if (WARM) { const int pi = 5 * mem;
        if (w == 0) touch4(XB + w4k, lane == 0 ? 16 * 81920 : OOB, mk_srd(part, 16 * 81920 + 4096), 0);
        if (w == 1) touch4(XB + w4k, lane == 0 ? (32 * (pi / 40) + 8 * (pi & 3)) * 1280 + ((pi % 40) >> 2) * 128 : OOB, mk_srd(out + b * 81920, 81920), 0); }
      bar_l();
      w_pair();
      STAMP(104);
      bar_l();
      STAMP(105);
      w_triple();
      bar_v<12>();
      STAMP(106);
      touch4(w4k, lane < 20 ? trow + (lane >> 1) * 12288 + (lane & 1) * 128 : OOB, sQ, 128);
      { const unsigned tl = b * 120 + w * 30 + lane;
        if (PTOUCH & 1) touch4(w4k, lane < 30 ? (tl / 3) * 12288 + (tl % 3) * 128 : OOB, sQ, 384);
        if (PTOUCH & 2) touch4(w4k, lane < 30 ? tl * 128 : OOB, sO, 0);
        if (PTOUCH & 4) touch4(w4k, lane < 30 ? tl * 128 : OOB, sO, 245760); }
      { const unsigned vK = v_k(lane); for (int u = 0; u < 2; ++u) dma4<true>(u * US + w4k, vK, vK ^ 64, sK, u * 49152, 3072); }
      STAMP(107);
      bar_v<9 + (PTOUCH & 1) + ((PTOUCH >> 1) & 1) + ((PTOUCH >> 2) & 1)>();
      bar_l();
      w_pair();
      if (PHASE_D) { const unsigned ph = PHASE_N == 2 ? (b & 1) : PHASE_N == 3 ? (b % 3) : PHASE_N == 5 ? (unsigned)hp : PHASE_N == 8 ? (b & 7) : (b & 3); const u64 t0_ = __builtin_amdgcn_s_memrealtime(); while (__builtin_amdgcn_s_memrealtime() - t0_ < ph * PHASE_D) __builtin_amdgcn_s_sleep(1); }
      bar_v<12>();
      STAMP(1); }
    const unsigned ka0 = r16 * 128 + ((g ^ ((r16 >> 1) & 7)) << 4);
    const unsigned va0 = (4 * g + q4) * 128 + p * 8 + (((2 * g + (q4 >> 1)) & 3) << 5);
    const unsigned wa0 = (16 * w + r16) * 128 + (((g >> 1) ^ ((r16 >> 1) & 7)) << 4) + (g & 1) * 8 + HB;
    bf8 qc[2], qn[3][2], pf[16]; f4 S[32], O[4], Ls; u2 hA[4]; float m0, mc[2], alpha, linv;
    const u4 ones_u = {0x3f803f80u, 0x3f803f80u, 0x3f803f80u, 0x3f803f80u};
    auto read_q = [&](bf8* q, int hs) { const unsigned a = opq(ka0 + HB + w * 2048 + hs * 8192); q[0] = ld128(sm, a); q[1] = ld128(sm, a ^ 64); };
    auto k_unit = [&](auto kb_, auto un_, auto first_) { constexpr int kb = decltype(kb_)::v, un = decltype(un_)::v, PD = 8; constexpr bool first = decltype(first_)::v;
      const unsigned a0 = opq(ka0) + kb * KVS + un * US, a1 = a0 ^ 64;
      bf8 a[16];
      auto rd = [&](auto i_) { constexpr int i = decltype(i_)::v; a[i] = ld128(sm, ((i & 1) ? a1 : a0) + (i >> 1) * 2048); };
      sfor<0, PD>(rd); SB();
      sfor<0, 8>([&](auto j_) { constexpr int j = decltype(j_)::v, blk = 16 * kb + 8 * un + j;
        f4 c; if constexpr (first) c = f4{0, 0, 0, 0}; else c = S[blk];
        c = mfma16(a[2 * j + 1], qc[1], c); S[blk] = mfma16(a[2 * j], qc[0], c);
        if constexpr (2 * j + PD < 16) { rd(IC<2 * j + PD>{}); rd(IC<2 * j + PD + 1>{}); }
        SB(); }); };
    auto max_half = [&](auto h_) { constexpr int h = decltype(h_)::v;
      float m = fmaxf(S[16 * h][0], S[16 * h][1]), m2 = fmaxf(S[16 * h][2], S[16 * h][3]);
      sfor<1, 16>([&](auto b_) { constexpr int i = 16 * h + decltype(b_)::v; m = fmaxf(fmaxf(m, S[i][0]), S[i][1]); m2 = fmaxf(fmaxf(m2, S[i][2]), S[i][3]); });
      m = fmaxf(m, m2);
      { auto r = __builtin_amdgcn_permlane16_swap(__float_as_uint(m), __float_as_uint(m), false, false); m = fmaxf(__uint_as_float(r[0]), __uint_as_float(r[1])); }
      { auto r = __builtin_amdgcn_permlane32_swap(__float_as_uint(m), __float_as_uint(m), false, false); m = fmaxf(__uint_as_float(r[0]), __uint_as_float(r[1])); }
      if constexpr (h == 0) m0 = m; else { m = fmaxf(m, m0); alpha = __builtin_amdgcn_exp2f((m0 - m) * SC); }
      mc[h] = -m * SC; };
    auto exp_kk = [&](auto kk_) { constexpr int kk = decltype(kk_)::v; float e[8];
      sfor<0, 8>([&](auto j_) { constexpr int j = decltype(j_)::v; e[j] = __builtin_amdgcn_exp2f(fmaf(S[2 * kk + (j >> 2)][j & 3], SC, mc[kk >> 3])); });
      u4 u = {pk(e[0], e[1]), pk(e[2], e[3]), pk(e[4], e[5]), pk(e[6], e[7])}; if (PIN) asm volatile("" : "+v"(u)); pf[kk] = __builtin_bit_cast(bf8, u); };
    auto v_unit = [&](auto kb_, auto un_, auto ls_) { constexpr int kb = decltype(kb_)::v, un = decltype(un_)::v, PD = 6; constexpr bool ls = decltype(ls_)::v;
      const unsigned a0 = opq(va0) + kb * KVS + un * US;
      bf8 a[16];
      auto rd = [&](auto i_) { constexpr int i = decltype(i_)::v; const unsigned ad = (a0 ^ ((i & 3) << 5)) + (i >> 2) * 4096; a[i] = ldtr(sm, ad, ad + 2048); };
      sfor<0, PD>(rd);
      if constexpr (kb == 1 && un == 0) { sfor<0, 4>([&](auto d_) { constexpr int db = decltype(d_)::v; O[db] *= alpha; }); if constexpr (ls) Ls *= alpha; }
      SB();
      sfor<0, 8>([&](auto j_) { constexpr int j = decltype(j_)::v, kk = 8 * kb + 4 * un + (j >> 1), d0 = (j & 1) * 2;
        f4 c0, c1; if constexpr (kk == 0) { c0 = f4{0, 0, 0, 0}; c1 = c0; } else { c0 = O[d0]; c1 = O[d0 + 1]; }
        O[d0 + 1] = mfma16(a[2 * j + 1], pf[kk], c1); O[d0] = mfma16(a[2 * j], pf[kk], c0);
        if constexpr (ls && (j & 1)) { f4 cl; if constexpr (kk == 0) cl = f4{0, 0, 0, 0}; else cl = Ls; Ls = mfma16(__builtin_bit_cast(bf8, ones_u), pf[kk], cl); }
        if constexpr (2 * j + PD < 16) { rd(IC<2 * j + PD>{}); rd(IC<2 * j + PD + 1>{}); }
        SB(); }); };
    auto finish_o = [&]() { sfor<0, 4>([&](auto d_) { constexpr int db = decltype(d_)::v;
        u2 t_ = u2{pk(O[db][0] * linv, O[db][1] * linv), pk(O[db][2] * linv, O[db][3] * linv)}; if (PIN) asm volatile("" : "+v"(t_)); hA[db] = t_; }); };
    auto write_o = [&](int hs) { const unsigned a = opq(wa0 + hs * 8192);
      sfor<0, 4>([&](auto d_) { constexpr int db = decltype(d_)::v; *(LAS u2*)(sm + (a ^ (db << 5))) = hA[db]; }); };
    sfor<0, 3>([&](auto i_) { qn[decltype(i_)::v][0] = bf8{}; qn[decltype(i_)::v][1] = bf8{}; });

    auto bar_a = [&](int c, bool second) { if ((((unsigned)WHOLE >> c) & 1) == (unsigned)second) { STAMP(6 + 4 * c); bar_v<0>(); } };
    const u64 tag = epoch + 1;
    auto w_and_c = [&](int c) { STAMP(5 + 4 * c);
      w_issue(c); STAMP(7 + 4 * c); bar_l(); };
#pragma clang loop unroll(disable)
    for (int it = 0; it < 2; ++it) {
      it = opqs(it);
      const int c0 = 12 * it;
      sfor<0, 6>([&](auto r_) { constexpr int r = decltype(r_)::v, ch = r >> 1, kb = r & 1;
        if constexpr (kb == 0) { bf8 n[2] = {qn[ch][0], qn[ch][1]}; if (it == 0) read_q(n, ch & 1); qc[0] = n[0]; qc[1] = n[1]; }
        k_unit(IC<kb>{}, IC<0>{}, IC<ch == 0>{});
        if constexpr (r == 5) sfor<1, 4>(exp_kk);
        bar_a(c0 + r, false);
        k_unit(IC<kb>{}, IC<1>{}, IC<ch == 0>{});
        bar_a(c0 + r, true);
        if constexpr (r == 4) { max_half(IC<0>{}); sfor<0, 1>(exp_kk); }
        if constexpr (r == 5) { sfor<4, 5>(exp_kk); max_half(IC<1>{}); }
        w_and_c(c0 + r); });
      if (it == 0) read_q(qn[0], 1);
      v_unit(IC<0>{}, IC<0>{}, IC<1>{}); sfor<5, 9>(exp_kk);
      bar_a(c0 + 6, false);
      v_unit(IC<0>{}, IC<1>{}, IC<1>{});
      bar_a(c0 + 6, true);
      sfor<9, 12>(exp_kk);
      w_and_c(c0 + 6);
      v_unit(IC<1>{}, IC<0>{}, IC<1>{}); sfor<12, 16>(exp_kk);
      bar_a(c0 + 7, false);
      v_unit(IC<1>{}, IC<1>{}, IC<1>{}); linv = 1.0f / Ls[0]; finish_o();
      if (it == 1) write_o(1);
      bar_a(c0 + 7, true);
      w_and_c(c0 + 7);
#pragma clang loop unroll(disable)
      for (int ch = 1; ch < 3; ++ch) {
        ch = opqs(ch);
        const int c = c0 + 6 + 2 * ch;
        if (it == 0) { bf8 n[2]; read_q(n, (ch + 1) & 1); qn[1][0] = qn[2][0]; qn[1][1] = qn[2][1]; qn[2][0] = n[0]; qn[2][1] = n[1]; }
        v_unit(IC<0>{}, IC<0>{}, IC<0>{});
        bar_a(c, false);
        v_unit(IC<0>{}, IC<1>{}, IC<0>{});
        bar_a(c, true);
        if (it == 0) write_o((ch - 1) & 1);
        w_and_c(c);
        if (c == 22) w_triple();
        v_unit(IC<1>{}, IC<0>{}, IC<0>{});
        if (c == 22) bar_l(); else bar_a(c + 1, false);
        v_unit(IC<1>{}, IC<1>{}, IC<0>{}); finish_o();
        if (it == 1 || ch == 2) write_o((it + ch) & 1);
        if (c == 22) break;
        bar_a(c + 1, true);
        w_and_c(c + 1);
      }
    }
    bar_v<12>();
    bar_v<0>();
    bar_l();
    if (w == 0) {
      const int l = lane_id(); bool ok = false;
      if (l == 0) __hip_atomic_store(sync + 32 + mem, tag, __ATOMIC_RELAXED, __HIP_MEMORY_SCOPE_AGENT);
      for (unsigned spins = 0; spins < (1u << 20) && !ok; ++spins) {
        u64 f = tag; if (l < 16) f = __hip_atomic_load(sync + 32 + l, __ATOMIC_RELAXED, __HIP_MEMORY_SCOPE_AGENT);
        ok = __all(f == tag); if (!ok) __builtin_amdgcn_s_sleep(1); }
      if (ok && l == 0 && mem == 0) __hip_atomic_store(sync, tag, __ATOMIC_RELAXED, __HIP_MEMORY_SCOPE_AGENT);
      __builtin_amdgcn_fence(__ATOMIC_ACQUIRE, "wavefront");
    }
    bar_l();
    reduce_slice(w, tag);
    STAMP_FLUSH();
  } else {
    __builtin_amdgcn_s_setprio(PROJ_PRIO);
    const int lane = lane_id();
    STAMP_DECL; STAMP(0);
    { const u4 sX = mk_srd(x + b * 81920, 81920);
      const unsigned vX = (16 * w + (lane >> 4)) * 1280 + (((lane & 15) ^ (lane >> 4)) << 4);
      const unsigned t0 = (hp ? HB : 0) + w4k;
      touch4(t0, lane < 10 ? (b * 40 + w * 10 + lane) * 12288 : OOB, sQ, 0);
      { u64 pc; asm volatile("s_getpc_b64 %0" : "=s"(pc));
        const u4 sC = {(unsigned)pc, (unsigned)(pc >> 32) & 0xffffu, CODE_BYTES, 0x00020000u};
        const u4 st = {w == 0 ? sK[0] : w == 1 ? sV[0] : w == 2 ? sO[0] : sC[0], w == 0 ? sK[1] : w == 1 ? sV[1] : w == 2 ? sO[1] : sC[1], w == 3 ? sC[2] : sK[2], sK[3]};
        touch4(t0, w == 3 ? (lane < 8 ? (idx * 8 + lane) * 128 : OOB) : lane < 2 ? lane * ITEM : OOB, st, 0); }
      for (int t = 0; t < 5; ++t) { const unsigned tt = hp ? 4 - t : t, lb = tt == 4 ? HB : tt * US;
        sfor<0, 4>([&](auto j_) { constexpr int j = decltype(j_)::v; dma1(lb + w4k + j * 1024, vX ^ (j << 6), sX, tt * 256 + j * 4 * 1280); }); }
      bar_l(); }
    const int qh = w & 1, ah = w >> 1, h = lane >> 5, b16 = (lane >> 4) & 1, q4 = (lane & 15) >> 2, p = lane & 3;
    const unsigned row = 32 * qh + (lane & 31), f8 = (row >> 1) & 7;
    const unsigned qa0 = (8 * h + q4) * 128 + ((ah ^ (q4 >> 1)) << 6) + 32 * b16 + 8 * p;
    const unsigned oa0 = (8 * h + q4) * 256 + (((2 * ah) ^ q4) << 6) + 32 * b16 + 8 * p;
    const unsigned ha0 = row * 128 + ((h ^ f8) << 4) + HB;
    constexpr int PD = 6;
    const unsigned vK = v_k(lane), vV = (32 * w + (lane >> 3)) * 384 + (((lane & 7) ^ (((lane >> 4) & 3) << 1)) << 4);
    unsigned k_so = 2 * 49152, k_n = 1, k_vE = vK, k_vO = vK ^ 64; u4 k_srd = sK;
    auto kv_tile = [&]() { const unsigned lb = (k_n & 1) * KVS + w4k;
      dma4<true>(lb, k_vE, k_vO, k_srd, k_so, 3072); dma4<true>(lb + US, k_vE, k_vO, k_srd, k_so + 49152, 3072);
      ++k_n; k_so += (k_n & 1) ? 2 * 49152u : 128u - 2 * 49152u;
      if (k_n == 6 || k_n == 12 || k_n == 18) { const bool isV = k_n != 12;
        k_so = k_n == 6 ? 0 : ITEM; k_srd[0] = sel(isV, sV[0], sK[0]); k_srd[1] = sel(isV, sV[1], sK[1]); k_vE = sel(isV, vV, vK); k_vO = sel(isV, vV, vK ^ 64); } };
    auto seam = [&](bool kv) { STAMP(k_n >= 2 ? 4 * k_n - 3 : 2);
      if (((unsigned)WHOLE >> (k_n - 1)) & 1) bar_v<0>(); else bar_v<4>();
      STAMP(k_n >= 2 ? 4 * k_n - 2 : 1);
      if (kv) {
        if (k_n == TQ || k_n == TO0 || k_n == TO1) { const unsigned l = lane_id(), tl = b * 120 + w * 30 + l, ld = (k_n & 1) * KVS + w4k;
          if (k_n == TQ) touch4(ld, l < 30 ? (tl / 3) * 12288 + (tl % 3) * 128 : OOB, sQ, 384);
          if (k_n == TO0) touch4(ld, l < 30 ? tl * 128 : OOB, sO, 0);
          if (k_n == TO1) touch4(ld, l < 30 ? tl * 128 : OOB, sO, 245760); }
        kv_tile(); STAMP(k_n >= 3 ? 4 * k_n - 5 : 2); bar_v<8>(); } else bar_v<0>();
      STAMPA(k_n >= 2 ? 4 * k_n - 4 : 2); };

#pragma clang loop unroll(disable)
    for (int pass = DRY ? 0 : 1; pass < 2; ++pass) {
    pass = opqs(pass);
    const bool dry = pass == 0;
    {
      bf8 xf[40]; f16v Qa = {};
      sfor<0, 40>([&](auto s_) { xf[decltype(s_)::v] = undef8(); });
      auto q_run = [&](auto t0_, auto n_, unsigned lb) { constexpr int T0 = decltype(t0_)::v, N = 8 * decltype(n_)::v;
        const unsigned a0 = opq(qa0 + lb);
        bf8 a[N];
        auto rd = [&](auto i_) { constexpr int i = decltype(i_)::v; a[i] = ldtr(sm, a0 + i * 2048, a0 + i * 2048 + 512); };
        sfor<0, PD>(rd); SB();
        sfor<0, N / 2>([&](auto j_) { constexpr int i = 2 * decltype(j_)::v;
          f16v c; if constexpr (T0 == 0 && i == 0) c = f16v{}; else c = Qa;
          c = mfma32(a[i + 1], xf[8 * T0 + i + 1], c); Qa = mfma32(a[i], xf[8 * T0 + i], c);
          if constexpr (i + PD < N) { rd(IC<i + PD>{}); rd(IC<i + PD + 1>{}); }
          SB(); }); };
      if (!dry) { bar_v<0>();
        STAMP(105);
        const unsigned xa = row * 256 + ((h ^ (row & 15)) << 4);
        sfor<0, 40>([&](auto s_) { constexpr int s = decltype(s_)::v; xf[s] = ld128(sm, (xa ^ ((s & 7) << 5)) + (s >= 32 ? HB : (s >> 3) * US)); });
        bar_l();
        STAMP(106); }
#pragma clang loop unroll(disable)
      for (int gq = 0; gq < 6; ++gq) {
        gq = opqs(gq);
        q_run(IC<0>{}, IC<2>{}, XB);
        if (!dry) seam(gq > 0);
        if (TRI_PRIO & 1) __builtin_amdgcn_s_setprio(2);
        q_run(IC<2>{}, IC<3>{}, YB);
        if (TRI_PRIO & 1) __builtin_amdgcn_s_setprio(0);
        if (dry) break;
        { const unsigned a = opq((ha0 ^ (h << 4)) + (gq & 1) * 8192 + 8 * h) ^ (ah << 6);
          sfor<0, 4>([&](auto i_) { constexpr int i = decltype(i_)::v;
            *(LAS u2*)(sm + (a ^ (i << 4))) = u2{pk(Qa[4 * i], Qa[4 * i + 1]), pk(Qa[4 * i + 2], Qa[4 * i + 3])}; }); }
        seam(true);
      }
    }
    {
      f16v acc[10]; bf8 ob[4];
      sfor<0, 10>([&](auto i_) { acc[decltype(i_)::v] = f16v{}; });
      const auto rsP = __builtin_amdgcn_make_buffer_rsrc(part + mem * 81920 + (qh * 40 + 4 * ah) * 1024, 0, dry ? 0 : 81920, 0x00020000);
      auto store_t = [&](auto t_) { constexpr int T = decltype(t_)::v; const unsigned l = lane_id(), pi = 40 * qh + 8 * T + 4 * ah + ((l & 31) >> 3) - 5 * mem; const bool own = OWN && pi < 5;
        sfor<0, 4>([&](auto i_) { constexpr int j = decltype(i_)::v >> 1, hf = decltype(i_)::v & 1; const f16v& a = acc[2 * T + j];
          u4 u = {pk(a[8 * hf], a[8 * hf + 1]), pk(a[8 * hf + 2], a[8 * hf + 3]), pk(a[8 * hf + 4], a[8 * hf + 5]), pk(a[8 * hf + 6], a[8 * hf + 7])};
          __builtin_amdgcn_raw_buffer_store_b128(u, rsP, own ? OOB : l * 16, ((4 * T + j) * 2 + hf) * 1024, PART_AUX);
          if (own) *(LAS u4*)(sm + pi * 1024 + (2 * j + hf) * 256 + (l >> 5) * 128 + (l & 7) * 16) = u; }); };
      auto o_run = [&](auto t0_, auto n_, unsigned lb, bool last) { constexpr int T0 = decltype(t0_)::v, N = 8 * decltype(n_)::v;
        const unsigned a0 = opq(oa0 + lb), a1 = a0 ^ 64;
        bf8 a[N];
        auto rd = [&](auto i_) { constexpr int i = decltype(i_)::v; const unsigned ad = ((i & 4) ? a1 : a0) + (i >> 3) * US + (i & 3) * 4096; a[i] = ldtr(sm, ad, ad + 1024); };
        sfor<0, PD>(rd); SB();
        sfor<0, N / 2>([&](auto j_) { constexpr int j = decltype(j_)::v, i = 2 * j, blk = 2 * T0 + (j >> 1), k = (j & 1) * 2;
          f16v c = mfma32(a[i + 1], ob[k + 1], acc[blk]); acc[blk] = mfma32(a[i], ob[k], c);
          if constexpr (i + PD < N) { rd(IC<i + PD>{}); rd(IC<i + PD + 1>{}); }
          SB();
          if constexpr ((j & 3) == 3) { if (last) store_t(IC<T0 + (j >> 2)>{}); }
          }); };
#pragma clang loop unroll(disable)
      for (int go = dry ? 5 : 0; go < 6; ++go) {
        go = opqs(go);
        const bool last = go == 5;
        { const unsigned ad = opq(ha0 + (go & 1) * 8192); sfor<0, 4>([&](auto k_) { constexpr int k = decltype(k_)::v; ob[k] = ld128(sm, ad ^ (k << 5)); }); }
        o_run(IC<0>{}, IC<2>{}, XB, last);
        if (!last) seam(true); else if (!dry) bar_l();
        if (TRI_PRIO & 2) __builtin_amdgcn_s_setprio(2);
        o_run(IC<2>{}, IC<3>{}, YB, last);
        if (TRI_PRIO & 2) __builtin_amdgcn_s_setprio(0);
        if (last) break;
        seam(go != 4);
        if (go < 3) seam(true);
        if (go == 4) { STAMP(100); bar_l(); STAMP(101); }
      }
    }
    }
    STAMP(102);
    bar_v<0>();
    STAMP(103);
    bar_l();
    STAMP(124);
    if (w == 0) reduce_slice(4, epoch + 1);
    STAMP_FLUSH();
  }
}

extern "C" void kernel_launch(void* const* d_in, const int* in_sizes, int n_in, void* d_out, int out_size, void* d_ws, size_t ws_size, hipStream_t stream) {
  fused_kernel<<<256, 512, 0, stream>>>((const char*)d_in[0], (const char*)d_in[1], (const char*)d_in[2], (const char*)d_in[3], (const char*)d_in[4], (char*)d_ws, (char*)d_out
  );
}
```

```cpp
#include <hip/hip_runtime.h>
#include <hip/hip_bf16.h>
#include <stdint.h>

typedef short s4 __attribute__((ext_vector_type(4)));
typedef short s8 __attribute__((ext_vector_type(8)));
typedef __bf16 bf8 __attribute__((ext_vector_type(8)));
typedef __bf16 bf2 __attribute__((ext_vector_type(2)));
typedef float f2 __attribute__((ext_vector_type(2)));
typedef float f4 __attribute__((ext_vector_type(4)));
typedef float f16v __attribute__((ext_vector_type(16)));
typedef unsigned u2 __attribute__((ext_vector_type(2)));
typedef unsigned u4 __attribute__((ext_vector_type(4)));
typedef unsigned long long u64;
#define LAS __attribute__((address_space(3)))
#define GAS __attribute__((address_space(1)))
#define DI __device__ __forceinline__

template <int N> struct IC { static constexpr int v = N; };
template <int A, int B, class F> DI void sfor(F&& f) { if constexpr (A < B) { f(IC<A>{}); sfor<A + 1, B>(f); } }

constexpr unsigned US = 16384, KVS = 32768, XB = 65536, YB = 98304, HB = 147456, LDS_BYTES = 163840;
constexpr unsigned REG = 16 * 81920 + 8192;
constexpr unsigned ITEM = 196608;
constexpr float SC = 0.07216878364870322f * 1.4426950408889634f;

constexpr unsigned CODE_BYTES = 30000;
constexpr unsigned PHASE_TICKS = 80;
constexpr int SC1 = 16;

DI u4 mk_srd(const void* p, unsigned bytes) {
  const u64 a = (u64)p;
  u4 r = {(unsigned)__builtin_amdgcn_readfirstlane((unsigned)a), (unsigned)__builtin_amdgcn_readfirstlane((unsigned)(a >> 32) & 0xffffu), bytes, 0x00020000u};
  return r;
}
template <bool NT> DI void dma4(unsigned mb, unsigned v0, unsigned v1, u4 srd, unsigned so, unsigned st) {
  unsigned t;
#define DMA4_BODY(POL) \
  asm volatile("s_mov_b32 m0, %1\n\ts_add_u32 %0, %5, %6\n\tbuffer_load_dwordx4 %2, %4, %5 offen " POL "lds\n\t" \
               "s_add_u32 m0, %1, 0x400\n\ts_nop 0\n\tbuffer_load_dwordx4 %3, %4, %0 offen " POL "lds\n\t" \
               "s_add_u32 m0, %1, 0x800\n\ts_add_u32 %0, %0, %6\n\tbuffer_load_dwordx4 %2, %4, %0 offen " POL "lds\n\t" \
               "s_add_u32 m0, %1, 0xc00\n\ts_add_u32 %0, %0, %6\n\tbuffer_load_dwordx4 %3, %4, %0 offen " POL "lds" \
               : "=&s"(t) : "s"(mb), "v"(v0), "v"(v1), "s"(srd), "s"(so), "s"(st) : "memory", "scc")
  if constexpr (NT) DMA4_BODY("nt "); else DMA4_BODY("");
#undef DMA4_BODY
}
DI void dma1(unsigned m0v, unsigned voff, u4 srd, unsigned so) {
  asm volatile("s_mov_b32 m0, %0\n\ts_nop 0\n\tbuffer_load_dwordx4 %1, %2, %3 offen lds" :: "s"(m0v), "v"(voff), "s"(srd), "s"(so) : "memory");
}
DI void touch4(unsigned m0v, unsigned voff, u4 srd, unsigned so) {
  asm volatile("s_mov_b32 m0, %0\n\ts_nop 0\n\tbuffer_load_dword %1, %2, %3 offen lds" :: "s"(m0v), "v"(voff), "s"(srd), "s"(so) : "memory");
}
#define SB() __builtin_amdgcn_sched_barrier(0)
template <int N> DI void bar_v() { SB(); asm volatile("s_waitcnt vmcnt(%0) lgkmcnt(0)\n\ts_barrier" ::"i"(N) : "memory"); SB(); }
DI void bar_l() { SB(); asm volatile("s_waitcnt lgkmcnt(0)\n\ts_barrier" ::: "memory"); SB(); }

DI bf8 ld128(LAS char* sm, unsigned a) { return __builtin_bit_cast(bf8, *(LAS s8*)(sm + a)); }
DI bf8 ldtr(LAS char* sm, unsigned a0, unsigned a1) {
  s4 x = __builtin_amdgcn_ds_read_tr16_b64_v4i16((LAS s4*)(sm + a0)), y = __builtin_amdgcn_ds_read_tr16_b64_v4i16((LAS s4*)(sm + a1));
  return __builtin_bit_cast(bf8, __builtin_shufflevector(x, y, 0, 1, 2, 3, 4, 5, 6, 7));
}
DI unsigned opq(unsigned v) { asm volatile("" : "+v"(v)); return v; }
DI unsigned sel(bool c, unsigned a, unsigned b) { return c ? a : b; }
DI int opqs(int v) { v = __builtin_amdgcn_readfirstlane(v); asm volatile("" : "+s"(v)); return v; }
DI int lane_id() { unsigned z = 0; asm volatile("" : "+v"(z)); return __builtin_amdgcn_mbcnt_hi(~0u, __builtin_amdgcn_mbcnt_lo(~0u, z)); }
DI unsigned pk(float a, float b) { f2 x = {a, b}; return __builtin_bit_cast(unsigned, __builtin_convertvector(x, bf2)); }
DI f4 mfma16(bf8 a, bf8 b, f4 c) { return __builtin_amdgcn_mfma_f32_16x16x32_bf16(a, b, c, 0, 0, 0); }
DI f16v mfma32(bf8 a, bf8 b, f16v c) { return __builtin_amdgcn_mfma_f32_32x32x16_bf16(a, b, c, 0, 0, 0); }

constexpr u64 w_table() { u64 t = 0; for (int c = 0; c < 24; ++c) { const int n = c < 12 ? 1 + (c & 1) : (c == 13 || c == 16 || c == 19 || c == 21 || c == 23) ? 1 : (c == 14 || c == 17 || c == 20 || c == 22) ? 2 : 0; t |= (u64)n << (2 * c); } return t; }
constexpr unsigned WHOLE = (1u << 19) | (1u << 21);

__global__ __launch_bounds__(512, 2) void fused_kernel(const char* __restrict__ x, const char* __restrict__ K, const char* __restrict__ V,
                                                       const char* __restrict__ wq, const char* __restrict__ wo, char* ws, char* __restrict__ out) {
  __shared__ __attribute__((aligned(16))) char smem[LDS_BYTES];
  LAS char* sm = (LAS char*)smem;
  const int wave = __builtin_amdgcn_readfirstlane(threadIdx.x >> 6), w = wave & 3;
  const int xcd = blockIdx.x & 7, idx = blockIdx.x >> 3, b = idx >> 1, hp = idx & 1, h0 = xcd * 4 + hp * 2, mem = xcd * 2 + hp;
  const u4 sK = mk_srd(K + (size_t)(b * 32 + h0) * ITEM, 2 * ITEM), sV = mk_srd(V + (size_t)(b * 32 + h0) * ITEM, 2 * ITEM);
  const u4 sQ = mk_srd(wq + h0 * 384, 640 * 12288 - h0 * 384), sO = mk_srd(wo + h0 * (192 * 1280), 384 * 1280);
  char* const part = ws + (size_t)b * REG + 4096;
  GAS u64* const sync = (GAS u64*)(ws + (size_t)b * REG);
  const unsigned w4k = w * 4096u, OOB = 0x7ffffff0u;
  auto v_k = [&](int l) { return (unsigned)((32 * w + (l >> 3)) * 384 + (((l & 7) ^ (l >> 4)) << 4)); };

  auto reduce_slice = [&](int i) {
    const int r = lane_id(), pi = 5 * mem + i, qh = pi / 40, L = (pi % 40) >> 2, o = pi & 3, un = r >> 4, h = (r >> 3) & 1, q8 = r & 7;
    const int nblk = 2 * L + (un >> 1), hf = un & 1;
    const auto rs = __builtin_amdgcn_make_buffer_rsrc(part, 0, 16 * 81920, 0x00020000);
    const unsigned vo = ((qh * 20 + nblk) * 2 + hf) * 1024 + (32 * h + 8 * o + q8) * 16;
    u4 v[16];
    sfor<0, 16>([&](auto p_) { constexpr int p = decltype(p_)::v; v[p] = __builtin_amdgcn_raw_buffer_load_b128(rs, vo, p * 81920, SC1); });
    float s[8] = {};
    sfor<0, 16>([&](auto p_) { constexpr int p = decltype(p_)::v; sfor<0, 4>([&](auto e_) { constexpr int e = decltype(e_)::v;
      s[2 * e] += __uint_as_float(v[p][e] << 16); s[2 * e + 1] += __uint_as_float(v[p][e] & 0xffff0000u); }); });
    const u2 lo = {pk(s[0], s[1]), pk(s[2], s[3])}, hi = {pk(s[4], s[5]), pk(s[6], s[7])};
    const unsigned s0 = h ? lo[0] : hi[0], s1 = h ? lo[1] : hi[1];
    const unsigned r0 = __builtin_amdgcn_update_dpp(0u, s0, 0x128, 0xf, 0xf, false), r1 = __builtin_amdgcn_update_dpp(0u, s1, 0x128, 0xf, 0xf, false);
    *(u4*)(out + (size_t)(b * 64 + 32 * qh + 8 * o + q8) * 1280 + (32 * nblk + 16 * hf + 8 * h) * 2) = h ? u4{r0, r1, hi[0], hi[1]} : u4{lo[0], lo[1], r0, r1}; };

  if (wave < 4) {
    const int lane = lane_id();
    const int r16 = lane & 15, g = lane >> 4, q4 = (lane & 15) >> 2, p = lane & 3;
    const u64 epoch = __hip_atomic_load(sync, __ATOMIC_RELAXED, __HIP_MEMORY_SCOPE_AGENT);
    unsigned w_so = 0, w_g = 0, w_v = (32 * w + (lane >> 3)) * 12288 + (((lane & 7) ^ (((lane >> 4) & 1) << 2)) << 4), w_st = 8 * 12288, w_dT = 128 * 12288, w_dG = 128u - 5u * 128 * 12288;
    u4 w_srd = sQ;
    auto w_tile = [&](unsigned lb) { dma4<false>(lb + w4k, w_v, w_v, w_srd, w_so, w_st); w_so += w_dT; };
    auto w_pair = [&]() { w_tile(XB); w_tile(XB + US); };
    auto w_triple = [&]() { w_tile(YB); w_tile(YB + US); w_tile(YB + 2 * US); w_so += w_dG;
      if (++w_g == 6) { const int l = lane_id(); w_so = 0; w_v = (16 * w + (l >> 4)) * 1280 + (((l & 15) ^ ((l >> 4) << 2)) << 4); w_st = 4 * 1280; w_dT = 256; w_dG = 64 * 1280 - 5 * 256; w_srd = sO; } };
    auto w_issue = [&](int c) { const unsigned n = (unsigned)(w_table() >> (2 * c + 2)) & 3; if (n & 1) w_pair(); if (n & 2) w_triple(); };
    { const unsigned trow = (b * 40 + w * 10) * 12288;
      { const int pi = 5 * mem;
        if (w == 0) touch4(XB + w4k, lane == 0 ? 16 * 81920 : OOB, mk_srd(part, 16 * 81920 + 4096), 0);
        if (w == 1) touch4(XB + w4k, lane == 0 ? (32 * (pi / 40) + 8 * (pi & 3)) * 1280 + ((pi % 40) >> 2) * 128 : OOB, mk_srd(out + b * 81920, 81920), 0); }
      bar_l();
      w_pair();
      bar_l();
      w_triple();
      bar_v<12>();
      touch4(w4k, lane < 20 ? trow + (lane >> 1) * 12288 + (lane & 1) * 128 : OOB, sQ, 128);
      { const unsigned vK = v_k(lane); for (int u = 0; u < 2; ++u) dma4<true>(u * US + w4k, vK, vK ^ 64, sK, u * 49152, 3072); }
      bar_v<9>();
      bar_l();
      w_pair();
      { const u64 t0_ = __builtin_amdgcn_s_memrealtime(); while (__builtin_amdgcn_s_memrealtime() - t0_ < (b & 1) * PHASE_TICKS) __builtin_amdgcn_s_sleep(1); }
      bar_v<12>();
    }
    const unsigned ka0 = r16 * 128 + ((g ^ ((r16 >> 1) & 7)) << 4);
    const unsigned va0 = (4 * g + q4) * 128 + p * 8 + (((2 * g + (q4 >> 1)) & 3) << 5);
    const unsigned wa0 = (16 * w + r16) * 128 + (((g >> 1) ^ ((r16 >> 1) & 7)) << 4) + (g & 1) * 8 + HB;
    bf8 qc[2], qn[3][2], pf[16]; f4 S[32], O[4], Ls; u2 hA[4]; float m0, mc[2], alpha, linv;
    const u4 ones_u = {0x3f803f80u, 0x3f803f80u, 0x3f803f80u, 0x3f803f80u};
    auto read_q = [&](bf8* q, int hs) { const unsigned a = opq(ka0 + HB + w * 2048 + hs * 8192); q[0] = ld128(sm, a); q[1] = ld128(sm, a ^ 64); };
    auto k_unit = [&](auto kb_, auto un_, auto first_) { constexpr int kb = decltype(kb_)::v, un = decltype(un_)::v, PD = 8; constexpr bool first = decltype(first_)::v;
      const unsigned a0 = opq(ka0) + kb * KVS + un * US, a1 = a0 ^ 64;
      bf8 a[16];
      auto rd = [&](auto i_) { constexpr int i = decltype(i_)::v; a[i] = ld128(sm, ((i & 1) ? a1 : a0) + (i >> 1) * 2048); };
      sfor<0, PD>(rd); SB();
      sfor<0, 8>([&](auto j_) { constexpr int j = decltype(j_)::v, blk = 16 * kb + 8 * un + j;
        f4 c; if constexpr (first) c = f4{0, 0, 0, 0}; else c = S[blk];
        c = mfma16(a[2 * j + 1], qc[1], c); S[blk] = mfma16(a[2 * j], qc[0], c);
        if constexpr (2 * j + PD < 16) { rd(IC<2 * j + PD>{}); rd(IC<2 * j + PD + 1>{}); }
        SB(); }); };
    auto max_half = [&](auto h_) { constexpr int h = decltype(h_)::v;
      float m = fmaxf(S[16 * h][0], S[16 * h][1]), m2 = fmaxf(S[16 * h][2], S[16 * h][3]);
      sfor<1, 16>([&](auto b_) { constexpr int i = 16 * h + decltype(b_)::v; m = fmaxf(fmaxf(m, S[i][0]), S[i][1]); m2 = fmaxf(fmaxf(m2, S[i][2]), S[i][3]); });
      m = fmaxf(m, m2);
      { auto r = __builtin_amdgcn_permlane16_swap(__float_as_uint(m), __float_as_uint(m), false, false); m = fmaxf(__uint_as_float(r[0]), __uint_as_float(r[1])); }
      { auto r = __builtin_amdgcn_permlane32_swap(__float_as_uint(m), __float_as_uint(m), false, false); m = fmaxf(__uint_as_float(r[0]), __uint_as_float(r[1])); }
      if constexpr (h == 0) m0 = m; else { m = fmaxf(m, m0); alpha = __builtin_amdgcn_exp2f((m0 - m) * SC); }
      mc[h] = -m * SC; };
    auto exp_kk = [&](auto kk_) { constexpr int kk = decltype(kk_)::v; float e[8];
      sfor<0, 8>([&](auto j_) { constexpr int j = decltype(j_)::v; e[j] = __builtin_amdgcn_exp2f(fmaf(S[2 * kk + (j >> 2)][j & 3], SC, mc[kk >> 3])); });
      u4 u = {pk(e[0], e[1]), pk(e[2], e[3]), pk(e[4], e[5]), pk(e[6], e[7])}; asm volatile("" : "+v"(u)); pf[kk] = __builtin_bit_cast(bf8, u); };
    auto v_unit = [&](auto kb_, auto un_, auto ls_) { constexpr int kb = decltype(kb_)::v, un = decltype(un_)::v, PD = 6; constexpr bool ls = decltype(ls_)::v;
      const unsigned a0 = opq(va0) + kb * KVS + un * US;
      bf8 a[16];
      auto rd = [&](auto i_) { constexpr int i = decltype(i_)::v; const unsigned ad = (a0 ^ ((i & 3) << 5)) + (i >> 2) * 4096; a[i] = ldtr(sm, ad, ad + 2048); };
      sfor<0, PD>(rd);
      if constexpr (kb == 1 && un == 0) { sfor<0, 4>([&](auto d_) { constexpr int db = decltype(d_)::v; O[db] *= alpha; }); if constexpr (ls) Ls *= alpha; }
      SB();
      sfor<0, 8>([&](auto j_) { constexpr int j = decltype(j_)::v, kk = 8 * kb + 4 * un + (j >> 1), d0 = (j & 1) * 2;
        f4 c0, c1; if constexpr (kk == 0) { c0 = f4{0, 0, 0, 0}; c1 = c0; } else { c0 = O[d0]; c1 = O[d0 + 1]; }
        O[d0 + 1] = mfma16(a[2 * j + 1], pf[kk], c1); O[d0] = mfma16(a[2 * j], pf[kk], c0);
        if constexpr (ls && (j & 1)) { f4 cl; if constexpr (kk == 0) cl = f4{0, 0, 0, 0}; else cl = Ls; Ls = mfma16(__builtin_bit_cast(bf8, ones_u), pf[kk], cl); }
        if constexpr (2 * j + PD < 16) { rd(IC<2 * j + PD>{}); rd(IC<2 * j + PD + 1>{}); }
        SB(); }); };
    auto finish_o = [&]() { sfor<0, 4>([&](auto d_) { constexpr int db = decltype(d_)::v;
        u2 t_ = u2{pk(O[db][0] * linv, O[db][1] * linv), pk(O[db][2] * linv, O[db][3] * linv)}; asm volatile("" : "+v"(t_)); hA[db] = t_; }); };
    auto write_o = [&](int hs) { const unsigned a = opq(wa0 + hs * 8192);
      sfor<0, 4>([&](auto d_) { constexpr int db = decltype(d_)::v; *(LAS u2*)(sm + (a ^ (db << 5))) = hA[db]; }); };
    sfor<0, 3>([&](auto i_) { qn[decltype(i_)::v][0] = bf8{}; qn[decltype(i_)::v][1] = bf8{}; });

    auto bar_a = [&](int c, bool second) { if (((WHOLE >> c) & 1) == (unsigned)second) { bar_v<0>(); } };
    const u64 tag = epoch + 1;
    auto w_and_c = [&](int c) { w_issue(c); bar_l(); };
#pragma clang loop unroll(disable)
    for (int it = 0; it < 2; ++it) {
      it = opqs(it);
      const int c0 = 12 * it;
      sfor<0, 6>([&](auto r_) { constexpr int r = decltype(r_)::v, ch = r >> 1, kb = r & 1;
        if constexpr (kb == 0) { bf8 n[2] = {qn[ch][0], qn[ch][1]}; if (it == 0) read_q(n, ch & 1); qc[0] = n[0]; qc[1] = n[1]; }
        k_unit(IC<kb>{}, IC<0>{}, IC<ch == 0>{});
        if constexpr (r == 5) sfor<1, 4>(exp_kk);
        bar_a(c0 + r, false);
        k_unit(IC<kb>{}, IC<1>{}, IC<ch == 0>{});
        bar_a(c0 + r, true);
        if constexpr (r == 4) { max_half(IC<0>{}); sfor<0, 1>(exp_kk); }
        if constexpr (r == 5) { sfor<4, 5>(exp_kk); max_half(IC<1>{}); }
        w_and_c(c0 + r); });
      if (it == 0) read_q(qn[0], 1);
      v_unit(IC<0>{}, IC<0>{}, IC<1>{}); sfor<5, 9>(exp_kk);
      bar_a(c0 + 6, false);
      v_unit(IC<0>{}, IC<1>{}, IC<1>{});
      bar_a(c0 + 6, true);
      sfor<9, 12>(exp_kk);
      w_and_c(c0 + 6);
      v_unit(IC<1>{}, IC<0>{}, IC<1>{}); sfor<12, 16>(exp_kk);
      bar_a(c0 + 7, false);
      v_unit(IC<1>{}, IC<1>{}, IC<1>{}); linv = 1.0f / Ls[0]; finish_o();
      if (it == 1) write_o(1);
      bar_a(c0 + 7, true);
      w_and_c(c0 + 7);
#pragma clang loop unroll(disable)
      for (int ch = 1; ch < 3; ++ch) {
        ch = opqs(ch);
        const int c = c0 + 6 + 2 * ch;
        if (it == 0) { bf8 n[2]; read_q(n, (ch + 1) & 1); qn[1][0] = qn[2][0]; qn[1][1] = qn[2][1]; qn[2][0] = n[0]; qn[2][1] = n[1]; }
        v_unit(IC<0>{}, IC<0>{}, IC<0>{});
        bar_a(c, false);
        v_unit(IC<0>{}, IC<1>{}, IC<0>{});
        bar_a(c, true);
        if (it == 0) write_o((ch - 1) & 1);
        w_and_c(c);
        if (c == 22) w_triple();
        v_unit(IC<1>{}, IC<0>{}, IC<0>{});
        if (c == 22) bar_l(); else bar_a(c + 1, false);
        v_unit(IC<1>{}, IC<1>{}, IC<0>{}); finish_o();
        if (it == 1 || ch == 2) write_o((it + ch) & 1);
        if (c == 22) break;
        bar_a(c + 1, true);
        w_and_c(c + 1);
      }
    }
    bar_v<12>();
    bar_v<0>();
    bar_l();
    if (w == 0) {
      const int l = lane_id(); bool ok = false;
      if (l == 0) __hip_atomic_store(sync + 32 + mem, tag, __ATOMIC_RELAXED, __HIP_MEMORY_SCOPE_AGENT);
      for (unsigned spins = 0; spins < (1u << 20) && !ok; ++spins) {
        u64 f = tag; if (l < 16) f = __hip_atomic_load(sync + 32 + l, __ATOMIC_RELAXED, __HIP_MEMORY_SCOPE_AGENT);
        ok = __all(f == tag); if (!ok) __builtin_amdgcn_s_sleep(1); }
      if (ok && l == 0 && mem == 0) __hip_atomic_store(sync, tag, __ATOMIC_RELAXED, __HIP_MEMORY_SCOPE_AGENT);
      __builtin_amdgcn_fence(__ATOMIC_ACQUIRE, "wavefront");
    }
    bar_l();
    reduce_slice(w);
  } else {
    const int lane = lane_id();
    { const u4 sX = mk_srd(x + b * 81920, 81920);
      const unsigned vX = (16 * w + (lane >> 4)) * 1280 + (((lane & 15) ^ (lane >> 4)) << 4);
      const unsigned t0 = (hp ? HB : 0) + w4k;
      touch4(t0, lane < 10 ? (b * 40 + w * 10 + lane) * 12288 : OOB, sQ, 0);
      { u64 pc; asm volatile("s_getpc_b64 %0" : "=s"(pc));
        const u4 sC = {(unsigned)pc, (unsigned)(pc >> 32) & 0xffffu, CODE_BYTES, 0x00020000u};
        const u4 st = {w == 0 ? sK[0] : w == 1 ? sV[0] : w == 2 ? sO[0] : sC[0], w == 0 ? sK[1] : w == 1 ? sV[1] : w == 2 ? sO[1] : sC[1], w == 3 ? sC[2] : sK[2], sK[3]};
        touch4(t0, w == 3 ? (lane < 8 ? (idx * 8 + lane) * 128 : OOB) : lane < 2 ? lane * ITEM : OOB, st, 0); }
      for (int t = 0; t < 5; ++t) { const unsigned tt = hp ? 4 - t : t, lb = tt == 4 ? HB : tt * US;
        sfor<0, 4>([&](auto j_) { constexpr int j = decltype(j_)::v; dma1(lb + w4k + j * 1024, vX ^ (j << 6), sX, tt * 256 + j * 4 * 1280); }); }
      bar_l(); }
    const int qh = w & 1, ah = w >> 1, h = lane >> 5, b16 = (lane >> 4) & 1, q4 = (lane & 15) >> 2, p = lane & 3;
    const unsigned row = 32 * qh + (lane & 31), f8 = (row >> 1) & 7;
    const unsigned qa0 = (8 * h + q4) * 128 + ((ah ^ (q4 >> 1)) << 6) + 32 * b16 + 8 * p;
    const unsigned oa0 = (8 * h + q4) * 256 + (((2 * ah) ^ q4) << 6) + 32 * b16 + 8 * p;
    const unsigned ha0 = row * 128 + ((h ^ f8) << 4) + HB;
    constexpr int PD = 6;
    const unsigned vK = v_k(lane), vV = (32 * w + (lane >> 3)) * 384 + (((lane & 7) ^ (((lane >> 4) & 3) << 1)) << 4);
    unsigned k_so = 2 * 49152, k_n = 1, k_vE = vK, k_vO = vK ^ 64; u4 k_srd = sK;
    auto kv_tile = [&]() { const unsigned lb = (k_n & 1) * KVS + w4k;
      dma4<true>(lb, k_vE, k_vO, k_srd, k_so, 3072); dma4<true>(lb + US, k_vE, k_vO, k_srd, k_so + 49152, 3072);
      ++k_n; k_so += (k_n & 1) ? 2 * 49152u : 128u - 2 * 49152u;
      if (k_n == 6 || k_n == 12 || k_n == 18) { const bool isV = k_n != 12;
        k_so = k_n == 6 ? 0 : ITEM; k_srd[0] = sel(isV, sV[0], sK[0]); k_srd[1] = sel(isV, sV[1], sK[1]); k_vE = sel(isV, vV, vK); k_vO = sel(isV, vV, vK ^ 64); } };
    auto seam = [&](bool kv) {
      if ((WHOLE >> (k_n - 1)) & 1) bar_v<0>(); else bar_v<4>();
      if (kv) {
        if (k_n == 2) { const unsigned l = lane_id(), tl = b * 120 + w * 30 + l;
          touch4(w4k, l < 30 ? (tl / 3) * 12288 + (tl % 3) * 128 : OOB, sQ, 384); touch4(w4k, l < 30 ? tl * 128 : OOB, sO, 0); touch4(w4k, l < 30 ? tl * 128 : OOB, sO, 245760); }
        kv_tile(); bar_v<8>(); } else bar_v<0>(); };

    {
      bf8 xf[40]; f16v Qa = {};
      auto q_run = [&](auto t0_, auto n_, unsigned lb) { constexpr int T0 = decltype(t0_)::v, N = 8 * decltype(n_)::v;
        const unsigned a0 = opq(qa0 + lb);
        bf8 a[N];
        auto rd = [&](auto i_) { constexpr int i = decltype(i_)::v; a[i] = ldtr(sm, a0 + i * 2048, a0 + i * 2048 + 512); };
        sfor<0, PD>(rd); SB();
        sfor<0, N / 2>([&](auto j_) { constexpr int i = 2 * decltype(j_)::v;
          f16v c; if constexpr (T0 == 0 && i == 0) c = f16v{}; else c = Qa;
          c = mfma32(a[i + 1], xf[8 * T0 + i + 1], c); Qa = mfma32(a[i], xf[8 * T0 + i], c);
          if constexpr (i + PD < N) { rd(IC<i + PD>{}); rd(IC<i + PD + 1>{}); }
          SB(); }); };
      bar_v<0>();
      { const unsigned xa = row * 256 + ((h ^ (row & 15)) << 4);
        sfor<0, 40>([&](auto s_) { constexpr int s = decltype(s_)::v; xf[s] = ld128(sm, (xa ^ ((s & 7) << 5)) + (s >= 32 ? HB : (s >> 3) * US)); }); }
      bar_l();
#pragma clang loop unroll(disable)
      for (int gq = 0; gq < 6; ++gq) {
        gq = opqs(gq);
        q_run(IC<0>{}, IC<2>{}, XB);
        seam(gq > 0);
        q_run(IC<2>{}, IC<3>{}, YB);
        { const unsigned a = opq((ha0 ^ (h << 4)) + (gq & 1) * 8192 + 8 * h) ^ (ah << 6);
          sfor<0, 4>([&](auto i_) { constexpr int i = decltype(i_)::v;
            *(LAS u2*)(sm + (a ^ (i << 4))) = u2{pk(Qa[4 * i], Qa[4 * i + 1]), pk(Qa[4 * i + 2], Qa[4 * i + 3])}; }); }
        seam(true);
      }
    }
    {
      f16v acc[10]; bf8 ob[4];
      sfor<0, 10>([&](auto i_) { acc[decltype(i_)::v] = f16v{}; });
      const auto rsP = __builtin_amdgcn_make_buffer_rsrc(part + mem * 81920 + (qh * 40 + 4 * ah) * 1024, 0, 81920, 0x00020000);
      auto store_t = [&](auto t_) { constexpr int T = decltype(t_)::v; const unsigned pv = lane_id() * 16;
        sfor<0, 4>([&](auto i_) { constexpr int j = decltype(i_)::v >> 1, hf = decltype(i_)::v & 1; const f16v& a = acc[2 * T + j];
          u4 u = {pk(a[8 * hf], a[8 * hf + 1]), pk(a[8 * hf + 2], a[8 * hf + 3]), pk(a[8 * hf + 4], a[8 * hf + 5]), pk(a[8 * hf + 6], a[8 * hf + 7])};
          __builtin_amdgcn_raw_buffer_store_b128(u, rsP, pv, ((4 * T + j) * 2 + hf) * 1024, SC1); }); };
      auto o_run = [&](auto t0_, auto n_, unsigned lb, bool last) { constexpr int T0 = decltype(t0_)::v, N = 8 * decltype(n_)::v;
        const unsigned a0 = opq(oa0 + lb), a1 = a0 ^ 64;
        bf8 a[N];
        auto rd = [&](auto i_) { constexpr int i = decltype(i_)::v; const unsigned ad = ((i & 4) ? a1 : a0) + (i >> 3) * US + (i & 3) * 4096; a[i] = ldtr(sm, ad, ad + 1024); };
        sfor<0, PD>(rd); SB();
        sfor<0, N / 2>([&](auto j_) { constexpr int j = decltype(j_)::v, i = 2 * j, blk = 2 * T0 + (j >> 1), k = (j & 1) * 2;
          f16v c = mfma32(a[i + 1], ob[k + 1], acc[blk]); acc[blk] = mfma32(a[i], ob[k], c);
          if constexpr (i + PD < N) { rd(IC<i + PD>{}); rd(IC<i + PD + 1>{}); }
          SB();
          if constexpr ((j & 3) == 3) { if (last) store_t(IC<T0 + (j >> 2)>{}); }
          }); };
#pragma clang loop unroll(disable)
      for (int go = 0; go < 6; ++go) {
        go = opqs(go);
        const bool last = go == 5;
        { const unsigned ad = opq(ha0 + (go & 1) * 8192); sfor<0, 4>([&](auto k_) { constexpr int k = decltype(k_)::v; ob[k] = ld128(sm, ad ^ (k << 5)); }); }
        o_run(IC<0>{}, IC<2>{}, XB, last);
        if (!last) seam(true); else bar_l();
        o_run(IC<2>{}, IC<3>{}, YB, last);
        if (last) break;
        seam(go != 4);
        if (go < 3) seam(true);
        if (go == 4) bar_l();
      }
    }
    bar_v<0>();
    bar_l();
    if (w == 0) reduce_slice(4);
  }
}

extern "C" void kernel_launch(void* const* d_in, const int* in_sizes, int n_in, void* d_out, int out_size, void* d_ws, size_t ws_size, hipStream_t stream) {
  fused_kernel<<<256, 512, 0, stream>>>((const char*)d_in[0], (const char*)d_in[1], (const char*)d_in[2], (const char*)d_in[3], (const char*)d_in[4], (char*)d_ws, (char*)d_out);
}
```

```cpp
#include <hip/hip_runtime.h>
#include <hip/hip_bf16.h>
#include <stdint.h>

typedef short s4 __attribute__((ext_vector_type(4)));
typedef short s8 __attribute__((ext_vector_type(8)));
typedef __bf16 bf8 __attribute__((ext_vector_type(8)));
typedef __bf16 bf2 __attribute__((ext_vector_type(2)));
typedef float f2 __attribute__((ext_vector_type(2)));
typedef float f4 __attribute__((ext_vector_type(4)));
typedef float f16v __attribute__((ext_vector_type(16)));
typedef unsigned u2 __attribute__((ext_vector_type(2)));
typedef unsigned u4 __attribute__((ext_vector_type(4)));
typedef unsigned long long u64;
#define LAS __attribute__((address_space(3)))
#define GAS __attribute__((address_space(1)))
#define DI __device__ __forceinline__

template <int N> struct IC { static constexpr int v = N; };
template <int A, int B, class F> DI void sfor(F&& f) { if constexpr (A < B) { f(IC<A>{}); sfor<A + 1, B>(f); } }

constexpr unsigned US = 16384, KVS = 32768, XB = 65536, YB = 98304, HB = 147456, LDS_BYTES = 163840;
constexpr unsigned REG = 16 * 81920 + 8192;
constexpr unsigned ITEM = 196608;
constexpr float SC = 0.07216878364870322f * 1.4426950408889634f;

constexpr unsigned CODE_BYTES = 27000;
constexpr int SC1 = 16;

DI u4 mk_srd(const void* p, unsigned bytes) {
  const u64 a = (u64)p;
  u4 r = {(unsigned)__builtin_amdgcn_readfirstlane((unsigned)a), (unsigned)__builtin_amdgcn_readfirstlane((unsigned)(a >> 32) & 0xffffu), bytes, 0x00020000u};
  return r;
}
template <bool NT> DI void dma4(unsigned mb, unsigned v0, unsigned v1, u4 srd, unsigned so, unsigned st) {
  unsigned t;
#define DMA4_BODY(POL) \
  asm volatile("s_mov_b32 m0, %1\n\ts_add_u32 %0, %5, %6\n\tbuffer_load_dwordx4 %2, %4, %5 offen " POL "lds\n\t" \
               "s_add_u32 m0, %1, 0x400\n\ts_nop 0\n\tbuffer_load_dwordx4 %3, %4, %0 offen " POL "lds\n\t" \
               "s_add_u32 m0, %1, 0x800\n\ts_add_u32 %0, %0, %6\n\tbuffer_load_dwordx4 %2, %4, %0 offen " POL "lds\n\t" \
               "s_add_u32 m0, %1, 0xc00\n\ts_add_u32 %0, %0, %6\n\tbuffer_load_dwordx4 %3, %4, %0 offen " POL "lds" \
               : "=&s"(t) : "s"(mb), "v"(v0), "v"(v1), "s"(srd), "s"(so), "s"(st) : "memory", "scc")
  if constexpr (NT) DMA4_BODY("nt "); else DMA4_BODY("");
#undef DMA4_BODY
}
DI void dma1(unsigned m0v, unsigned voff, u4 srd, unsigned so) {
  asm volatile("s_mov_b32 m0, %0\n\ts_nop 0\n\tbuffer_load_dwordx4 %1, %2, %3 offen lds" :: "s"(m0v), "v"(voff), "s"(srd), "s"(so) : "memory");
}
DI void touch4(unsigned m0v, unsigned voff, u4 srd, unsigned so) {
  asm volatile("s_mov_b32 m0, %0\n\ts_nop 0\n\tbuffer_load_dword %1, %2, %3 offen lds" :: "s"(m0v), "v"(voff), "s"(srd), "s"(so) : "memory");
}
DI void touchg(unsigned m0v, const char* p) {
  asm volatile("s_mov_b32 m0, %0\n\ts_nop 0\n\tglobal_load_lds_dword %1, off" :: "s"(m0v), "v"(p) : "memory");
}
#define SB() __builtin_amdgcn_sched_barrier(0)
template <int N> DI void bar_v() { SB(); asm volatile("s_waitcnt vmcnt(%0) lgkmcnt(0)\n\ts_barrier" ::"i"(N) : "memory"); SB(); }
DI void bar_l() { SB(); asm volatile("s_waitcnt lgkmcnt(0)\n\ts_barrier" ::: "memory"); SB(); }

DI bf8 ld128(LAS char* sm, unsigned a) { return __builtin_bit_cast(bf8, *(LAS s8*)(sm + a)); }
DI bf8 ldtr(LAS char* sm, unsigned a0, unsigned a1) {
  s4 x = __builtin_amdgcn_ds_read_tr16_b64_v4i16((LAS s4*)(sm + a0)), y = __builtin_amdgcn_ds_read_tr16_b64_v4i16((LAS s4*)(sm + a1));
  return __builtin_bit_cast(bf8, __builtin_shufflevector(x, y, 0, 1, 2, 3, 4, 5, 6, 7));
}
DI unsigned opq(unsigned v) { asm volatile("" : "+v"(v)); return v; }
DI unsigned sel(bool c, unsigned a, unsigned b) { return c ? a : b; }
DI int opqs(int v) { v = __builtin_amdgcn_readfirstlane(v); asm volatile("" : "+s"(v)); return v; }
DI int lane_id() { unsigned z = 0; asm volatile("" : "+v"(z)); return __builtin_amdgcn_mbcnt_hi(~0u, __builtin_amdgcn_mbcnt_lo(~0u, z)); }
DI unsigned pk(float a, float b) { f2 x = {a, b}; return __builtin_bit_cast(unsigned, __builtin_convertvector(x, bf2)); }
DI f4 mfma16(bf8 a, bf8 b, f4 c) { return __builtin_amdgcn_mfma_f32_16x16x32_bf16(a, b, c, 0, 0, 0); }
DI f16v mfma32(bf8 a, bf8 b, f16v c) { return __builtin_amdgcn_mfma_f32_32x32x16_bf16(a, b, c, 0, 0, 0); }


template <int A, int B> DI void w_tiles(unsigned lb, int g, int w, u4 sQ, u4 sO) {
  const int l = lane_id(); const bool o = g >= 6;
  const unsigned v = sel(o, (16 * w + (l >> 4)) * 1280 + (((l & 15) ^ ((l >> 4) << 2)) << 4), (32 * w + (l >> 3)) * 12288 + (((l & 7) ^ (((l >> 4) & 1) << 2)) << 4));
  const u4 srd = {sel(o, sO[0], sQ[0]), sel(o, sO[1], sQ[1]), sel(o, sO[2], sQ[2]), sQ[3]};
  const unsigned so = sel(o, (g - 6) * 81920, g * 128), dT = sel(o, 256, 128 * 12288), st = sel(o, 4 * 1280, 8 * 12288);
  sfor<A, B>([&](auto i_) { constexpr int i = decltype(i_)::v; dma4<false>(lb + (i - A) * US, v, v, srd, so + i * dT, st); });
}
template <int KB> DI void kv_tile(int n, int w, unsigned vK, unsigned vV, u4 sK, u4 sV) {
  const int ph = (n * 11) >> 5; const bool isV = ph & 1;
  const u4 srd = {sel(isV, sV[0], sK[0]), sel(isV, sV[1], sK[1]), sK[2], sK[3]};
  const unsigned so = (ph >> 1) * ITEM + KB * 2 * 49152 + (n - 3 * ph) * 128, vE = sel(isV, vV, vK), vO = sel(isV, vV, vK ^ 64), lb = KB * KVS + w * 4096;
  dma4<true>(lb, vE, vO, srd, so, 3072); dma4<true>(lb + US, vE, vO, srd, so + 49152, 3072);
}

__global__ __launch_bounds__(512, 2) void fused_kernel(const char* __restrict__ x, const char* __restrict__ K, const char* __restrict__ V,
                                                       const char* __restrict__ wq, const char* __restrict__ wo, char* ws, char* __restrict__ out) {
  __shared__ __attribute__((aligned(16))) char smem[LDS_BYTES];
  LAS char* sm = (LAS char*)smem;
  const int wave = __builtin_amdgcn_readfirstlane(threadIdx.x >> 6), w = wave & 3;
  const int xcd = blockIdx.x & 7, idx = blockIdx.x >> 3, b = idx >> 1, hp = idx & 1, h0 = xcd * 4 + hp * 2, mem = xcd * 2 + hp;
  const u4 sK = mk_srd(K + (size_t)(b * 32 + h0) * ITEM, 2 * ITEM), sV = mk_srd(V + (size_t)(b * 32 + h0) * ITEM, 2 * ITEM);
  const u4 sQ = mk_srd(wq + h0 * 384, 640 * 12288 - h0 * 384), sO = mk_srd(wo + h0 * (192 * 1280), 384 * 1280);
  char* const part = ws + (size_t)b * REG + 4096;
  GAS u64* const sync = (GAS u64*)(ws + (size_t)b * REG);
  const unsigned w4k = w * 4096u, OOB = 0x7ffffff0u;

  auto reduce_slice = [&](int i) {
    const int r = lane_id(), pi = 5 * mem + i, qh = pi / 40, L = (pi % 40) >> 2, o = pi & 3, un = r >> 4, h = (r >> 3) & 1, q8 = r & 7;
    const int nblk = 2 * L + (un >> 1), hf = un & 1;
    const auto rs = __builtin_amdgcn_make_buffer_rsrc(part, 0, 16 * 81920, 0x00020000);
    const unsigned vo = ((qh * 20 + nblk) * 2 + hf) * 1024 + (32 * h + 8 * o + q8) * 16;
    u4 v[16];
    sfor<0, 16>([&](auto p_) { constexpr int p = decltype(p_)::v; v[p] = __builtin_amdgcn_raw_buffer_load_b128(rs, vo, p * 81920, SC1); });
    float s[8] = {};
    sfor<0, 16>([&](auto p_) { constexpr int p = decltype(p_)::v; sfor<0, 4>([&](auto e_) { constexpr int e = decltype(e_)::v;
      s[2 * e] += __uint_as_float(v[p][e] << 16); s[2 * e + 1] += __uint_as_float(v[p][e] & 0xffff0000u); }); });
    const u2 lo = {pk(s[0], s[1]), pk(s[2], s[3])}, hi = {pk(s[4], s[5]), pk(s[6], s[7])};
    const unsigned s0 = h ? lo[0] : hi[0], s1 = h ? lo[1] : hi[1];
    const unsigned r0 = __builtin_amdgcn_update_dpp(0u, s0, 0x128, 0xf, 0xf, false), r1 = __builtin_amdgcn_update_dpp(0u, s1, 0x128, 0xf, 0xf, false);
    *(u4*)(out + (size_t)(b * 64 + 32 * qh + 8 * o + q8) * 1280 + (32 * nblk + 16 * hf + 8 * h) * 2) = h ? u4{r0, r1, hi[0], hi[1]} : u4{lo[0], lo[1], r0, r1}; };

  if (wave < 4) {
    const int lane = lane_id();
    const int r16 = lane & 15, g = lane >> 4, q4 = (lane & 15) >> 2, p = lane & 3;
    const unsigned vK = (32 * w + (lane >> 3)) * 384 + (((lane & 7) ^ (lane >> 4)) << 4), vV = (32 * w + (lane >> 3)) * 384 + (((lane & 7) ^ (((lane >> 4) & 3) << 1)) << 4);
    const u64 epoch = __hip_atomic_load(sync, __ATOMIC_RELAXED, __HIP_MEMORY_SCOPE_AGENT);
    { const unsigned trow = (b * 40 + w * 10) * 12288;
      { const int pi = 5 * mem;
        if (w == 0) touch4(XB + w4k, lane == 0 ? 16 * 81920 : OOB, mk_srd(part, 16 * 81920 + 4096), 0);
        if (w == 1) touch4(XB + w4k, lane == 0 ? (32 * (pi / 40) + 8 * (pi & 3)) * 1280 + ((pi % 40) >> 2) * 128 : OOB, mk_srd(out + b * 81920, 81920), 0); }
      bar_l();
      w_tiles<0, 2>(XB + w4k, 0, w, sQ, sO);
      bar_l();
      w_tiles<2, 5>(YB + w4k, 0, w, sQ, sO);
      bar_l();
      touch4(w4k, lane < 20 ? trow + (lane >> 1) * 12288 + (lane & 1) * 128 : OOB, sQ, 128);
      kv_tile<0>(0, w, vK, vV, sK, sV);
      bar_v<9>();
      {
        const unsigned ph = b & 7, first = 103 * ((b >> 3) * 28 + ph * (ph - 1) / 2), cnt = 103 * ph;
        for (unsigned i = w * 64; i < cnt; i += 256) { unsigned ln = first + min(i + lane, cnt - 1); ln = min(ln, 5759u);
          touchg(KVS + w4k, ln < 1920 ? wq + h0 * 384 + 384 + (ln / 3) * 12288 + (ln % 3) * 128 : wo + h0 * (192 * 1280) + (ln - 1920) * 128); } }
      bar_l();
    }
    const unsigned ka0 = r16 * 128 + ((g ^ ((r16 >> 1) & 7)) << 4);
    const unsigned va0 = (4 * g + q4) * 128 + p * 8 + (((2 * g + (q4 >> 1)) & 3) << 5);
    const unsigned wa0 = (16 * w + r16) * 128 + (((g >> 1) ^ ((r16 >> 1) & 7)) << 4) + (g & 1) * 8 + HB;
    bf8 qc[2], qn[3][2], pf[16]; f4 S[32], O[4], Ls; u2 hA[4]; float m0, mc[2], alpha, linv;
    const u4 ones_u = {0x3f803f80u, 0x3f803f80u, 0x3f803f80u, 0x3f803f80u};
    auto read_q = [&](bf8* q, int hs) { const unsigned a = opq(ka0 + HB + w * 2048 + hs * 8192); q[0] = ld128(sm, a); q[1] = ld128(sm, a ^ 64); };
    auto k_tile = [&](auto kb_, auto first_) { constexpr int kb = decltype(kb_)::v, PD = 8; constexpr bool first = decltype(first_)::v;
      const unsigned a0 = opq(ka0) + kb * KVS, a1 = a0 ^ 64;
      bf8 a[32];
      auto rd = [&](auto i_) { constexpr int i = decltype(i_)::v; a[i] = ld128(sm, ((i & 1) ? a1 : a0) + (i >> 1) * 2048); };
      sfor<0, PD>(rd); SB();
      sfor<0, 16>([&](auto j_) { constexpr int j = decltype(j_)::v, blk = 16 * kb + j;
        f4 c; if constexpr (first) c = f4{0, 0, 0, 0}; else c = S[blk];
        c = mfma16(a[2 * j + 1], qc[1], c); S[blk] = mfma16(a[2 * j], qc[0], c);
        if constexpr (2 * j + PD < 32) { rd(IC<2 * j + PD>{}); rd(IC<2 * j + PD + 1>{}); }
        SB(); }); };
    auto max_half = [&](auto h_) { constexpr int h = decltype(h_)::v;
      float m = fmaxf(S[16 * h][0], S[16 * h][1]), m2 = fmaxf(S[16 * h][2], S[16 * h][3]);
      sfor<1, 16>([&](auto b_) { constexpr int i = 16 * h + decltype(b_)::v; m = fmaxf(fmaxf(m, S[i][0]), S[i][1]); m2 = fmaxf(fmaxf(m2, S[i][2]), S[i][3]); });
      m = fmaxf(m, m2);
      { auto r = __builtin_amdgcn_permlane16_swap(__float_as_uint(m), __float_as_uint(m), false, false); m = fmaxf(__uint_as_float(r[0]), __uint_as_float(r[1])); }
      { auto r = __builtin_amdgcn_permlane32_swap(__float_as_uint(m), __float_as_uint(m), false, false); m = fmaxf(__uint_as_float(r[0]), __uint_as_float(r[1])); }
      if constexpr (h == 0) m0 = m; else { m = fmaxf(m, m0); alpha = __builtin_amdgcn_exp2f((m0 - m) * SC); }
      mc[h] = -m * SC; };
    auto exp_kk = [&](auto kk_) { constexpr int kk = decltype(kk_)::v; float e[8];
      sfor<0, 8>([&](auto j_) { constexpr int j = decltype(j_)::v; e[j] = __builtin_amdgcn_exp2f(fmaf(S[2 * kk + (j >> 2)][j & 3], SC, mc[kk >> 3])); });
      u4 u = {pk(e[0], e[1]), pk(e[2], e[3]), pk(e[4], e[5]), pk(e[6], e[7])}; asm volatile("" : "+v"(u)); pf[kk] = __builtin_bit_cast(bf8, u); };
    auto v_tile = [&](auto kb_, auto ls_) { constexpr int kb = decltype(kb_)::v, PD = 6; constexpr bool ls = decltype(ls_)::v;
      const unsigned a0 = opq(va0) + kb * KVS;
      bf8 a[32];
      auto rd = [&](auto i_) { constexpr int i = decltype(i_)::v; const unsigned ad = (a0 ^ ((i & 3) << 5)) + (i >> 2) * 4096; a[i] = ldtr(sm, ad, ad + 2048); };
      sfor<0, PD>(rd);
      if constexpr (kb == 1) { sfor<0, 4>([&](auto d_) { constexpr int db = decltype(d_)::v; O[db] *= alpha; }); if constexpr (ls) Ls *= alpha; }
      SB();
      sfor<0, 16>([&](auto j_) { constexpr int j = decltype(j_)::v, kk = 8 * kb + (j >> 1), d0 = (j & 1) * 2;
        f4 c0, c1; if constexpr (kk == 0) { c0 = f4{0, 0, 0, 0}; c1 = c0; } else { c0 = O[d0]; c1 = O[d0 + 1]; }
        O[d0 + 1] = mfma16(a[2 * j + 1], pf[kk], c1); O[d0] = mfma16(a[2 * j], pf[kk], c0);
        if constexpr (ls && (j & 1)) { f4 cl; if constexpr (kk == 0) cl = f4{0, 0, 0, 0}; else cl = Ls; Ls = mfma16(__builtin_bit_cast(bf8, ones_u), pf[kk], cl); }
        if constexpr (2 * j + PD < 32) { rd(IC<2 * j + PD>{}); rd(IC<2 * j + PD + 1>{}); }
        SB(); }); };
    auto finish_o = [&]() { sfor<0, 4>([&](auto d_) { constexpr int db = decltype(d_)::v;
        u2 t_ = u2{pk(O[db][0] * linv, O[db][1] * linv), pk(O[db][2] * linv, O[db][3] * linv)}; asm volatile("" : "+v"(t_)); hA[db] = t_; }); };
    auto write_o = [&](int hs) { const unsigned a = opq(wa0 + hs * 8192);
      sfor<0, 4>([&](auto d_) { constexpr int db = decltype(d_)::v; *(LAS u2*)(sm + (a ^ (db << 5))) = hA[db]; }); };
    sfor<0, 3>([&](auto i_) { qn[decltype(i_)::v][0] = bf8{}; qn[decltype(i_)::v][1] = bf8{}; });

    const u64 tag = epoch + 1;
    sfor<0, 4>([&](auto d_) { hA[decltype(d_)::v] = u2{0, 0}; });
#pragma clang loop unroll(disable)
    for (int it = 0; it < 2; ++it) {
      it = opqs(it);
      const int p0 = 6 * it; (void)p0;
      sfor<0, 3>([&](auto ch_) { constexpr int ch = decltype(ch_)::v;
        bar_v<0>();
        { bf8 n[2] = {qn[ch][0], qn[ch][1]}; if (it == 0) read_q(n, ch & 1); qc[0] = n[0]; qc[1] = n[1]; }
        if constexpr (ch == 0) { if (it == 1) write_o(0); }
        k_tile(IC<0>{}, IC<ch == 0>{});
        bar_l();
        kv_tile<0>(p0 + ch + 1, w, vK, vV, sK, sV);
        if constexpr (ch == 2) { max_half(IC<0>{}); sfor<0, 6>(exp_kk); }
        bar_l();
        k_tile(IC<1>{}, IC<ch == 0>{});
        if constexpr (ch == 2) { sfor<6, 8>(exp_kk); max_half(IC<1>{}); sfor<8, 11>(exp_kk); }
      });
      bar_v<0>();
      if (it == 0) read_q(qn[0], 1);
      v_tile(IC<0>{}, IC<1>{});
      bar_l();
      kv_tile<0>(p0 + 4, w, vK, vV, sK, sV);
      sfor<11, 16>(exp_kk);
      bar_l();
      v_tile(IC<1>{}, IC<1>{}); linv = 1.0f / Ls[0]; finish_o();
#pragma clang loop unroll(disable)
      for (int ch = 1; ch < 3; ++ch) {
        ch = opqs(ch);
        const int p = p0 + 3 + ch;
        bar_v<0>();
        if (it == 0) { bf8 n[2]; read_q(n, (ch + 1) & 1); qn[1][0] = qn[2][0]; qn[1][1] = qn[2][1]; qn[2][0] = n[0]; qn[2][1] = n[1]; }
        write_o((it + ch - 1) & 1);
        v_tile(IC<0>{}, IC<0>{});
        bar_l();
        if (p != 11) kv_tile<0>(p + 1, w, vK, vV, sK, sV);
        else w_tiles<0, 2>(w4k, 11, w, sQ, sO);
        bar_l();
        v_tile(IC<1>{}, IC<0>{}); finish_o();
      }
    }
    write_o(1);
    bar_v<0>();
    bar_l();
    if (w == 0) {
      const int l = lane_id(); bool ok = false;
      if (l == 0) __hip_atomic_store(sync + 32 + mem, tag, __ATOMIC_RELAXED, __HIP_MEMORY_SCOPE_AGENT);
      for (unsigned spins = 0; spins < (1u << 20) && !ok; ++spins) {
        u64 f = tag; if (l < 16) f = __hip_atomic_load(sync + 32 + l, __ATOMIC_RELAXED, __HIP_MEMORY_SCOPE_AGENT);
        ok = __all(f == tag); if (!ok) __builtin_amdgcn_s_sleep(1); }
      if (ok && l == 0 && mem == 0) __hip_atomic_store(sync, tag, __ATOMIC_RELAXED, __HIP_MEMORY_SCOPE_AGENT);
      __builtin_amdgcn_fence(__ATOMIC_ACQUIRE, "wavefront");
    }
    bar_l();
    reduce_slice(w);
  } else {
    const int lane = lane_id();
    { const u4 sX = mk_srd(x + b * 81920, 81920);
      const unsigned vX = (16 * w + (lane >> 4)) * 1280 + (((lane & 15) ^ (lane >> 4)) << 4);
      const unsigned t0 = (hp ? HB : 0) + w4k;
      touch4(t0, lane < 10 ? (b * 40 + w * 10 + lane) * 12288 : OOB, sQ, 0);
      { u64 pc; asm volatile("s_getpc_b64 %0" : "=s"(pc));
        const u4 sC = {(unsigned)pc, (unsigned)(pc >> 32) & 0xffffu, CODE_BYTES, 0x00020000u};
        const u4 st = {w == 0 ? sK[0] : w == 1 ? sV[0] : w == 2 ? sO[0] : sC[0], w == 0 ? sK[1] : w == 1 ? sV[1] : w == 2 ? sO[1] : sC[1], w == 3 ? sC[2] : sK[2], sK[3]};
        touch4(t0, w == 3 ? (lane < 8 ? (idx * 8 + lane) * 128 : OOB) : lane < 2 ? lane * ITEM : OOB, st, 0); }
      for (int t = 0; t < 5; ++t) { const unsigned tt = hp ? 4 - t : t, lb = tt == 4 ? HB : tt * US;
        sfor<0, 4>([&](auto j_) { constexpr int j = decltype(j_)::v; dma1(lb + w4k + j * 1024, vX ^ (j << 6), sX, tt * 256 + j * 4 * 1280); }); }
      bar_l(); }
    const int qh = w & 1, ah = w >> 1, h = lane >> 5, b16 = (lane >> 4) & 1, q4 = (lane & 15) >> 2, p = lane & 3;
    const unsigned row = 32 * qh + (lane & 31), f8 = (row >> 1) & 7;
    const unsigned qa0 = (8 * h + q4) * 128 + ((ah ^ (q4 >> 1)) << 6) + 32 * b16 + 8 * p;
    const unsigned oa0 = (8 * h + q4) * 256 + (((2 * ah) ^ q4) << 6) + 32 * b16 + 8 * p;
    const unsigned ha0 = row * 128 + ((h ^ f8) << 4) + HB;
    constexpr int PD = 6;
    const unsigned chunks = 0x1c3fu | (((unsigned)(0xc963u >> (4 * (b & 3))) & 15) << 6);
    const unsigned vK = (32 * w + (lane >> 3)) * 384 + (((lane & 7) ^ (lane >> 4)) << 4), vV = (32 * w + (lane >> 3)) * 384 + (((lane & 7) ^ (((lane >> 4) & 3) << 1)) << 4);
    int k_p = 0;
    int w_g = 1;
    auto seam = [&](auto lg_) { const int pp = k_p++;
      SB(); asm volatile("s_waitcnt vmcnt(0) lgkmcnt(%0)\n\ts_barrier" ::"i"(decltype(lg_)::v) : "memory"); SB();
      if (pp == 12) { w_tiles<2, 5>(YB + w4k, 11, w, sQ, sO); bar_v<0>(); return; }
      if ((chunks >> pp) & 1) { w_tiles<0, 5>(XB + w4k, w_g, w, sQ, sO); ++w_g; }
      bar_l();
      kv_tile<1>(pp, w, vK, vV, sK, sV); };

    {
      bf8 xf[40]; f16v Qa = {};
      auto q_run = [&](auto t0_, auto n_, unsigned lb) { constexpr int T0 = decltype(t0_)::v, N = 8 * decltype(n_)::v;
        const unsigned a0 = opq(qa0 + lb);
        bf8 a[N];
        auto rd = [&](auto i_) { constexpr int i = decltype(i_)::v; a[i] = ldtr(sm, a0 + i * 2048, a0 + i * 2048 + 512); };
        sfor<0, PD>(rd); SB();
        sfor<0, N / 2>([&](auto j_) { constexpr int i = 2 * decltype(j_)::v;
          f16v c; if constexpr (T0 == 0 && i == 0) c = f16v{}; else c = Qa;
          c = mfma32(a[i + 1], xf[8 * T0 + i + 1], c); Qa = mfma32(a[i], xf[8 * T0 + i], c);
          if constexpr (i + PD < N) { rd(IC<i + PD>{}); rd(IC<i + PD + 1>{}); }
          SB(); }); };
      { bar_v<0>();
        const unsigned xa = row * 256 + ((h ^ (row & 15)) << 4);
        sfor<0, 40>([&](auto s_) { constexpr int s = decltype(s_)::v; xf[s] = ld128(sm, (xa ^ ((s & 7) << 5)) + (s >= 32 ? HB : (s >> 3) * US)); });
        bar_l();
      }
#pragma clang loop unroll(disable)
      for (int gq = 0; gq < 6; ++gq) {
        gq = opqs(gq);
        bar_v<8>();
        q_run(IC<0>{}, IC<5>{}, XB);
        { const unsigned a = opq((ha0 ^ (h << 4)) + (gq & 1) * 8192 + 8 * h) ^ (ah << 6);
          sfor<0, 4>([&](auto i_) { constexpr int i = decltype(i_)::v;
            *(LAS u2*)(sm + (a ^ (i << 4))) = u2{pk(Qa[4 * i], Qa[4 * i + 1]), pk(Qa[4 * i + 2], Qa[4 * i + 3])}; }); }
        seam(IC<4>{});
      }
    }
    {
      f16v acc[10]; bf8 ob[4];
      sfor<0, 10>([&](auto i_) { acc[decltype(i_)::v] = f16v{}; });
      const auto rsP = __builtin_amdgcn_make_buffer_rsrc(part + mem * 81920 + (qh * 40 + 4 * ah) * 1024, 0, 81920, 0x00020000);
      auto store_t = [&](auto t_) { constexpr int T = decltype(t_)::v; const unsigned pv = lane_id() * 16;
        sfor<0, 4>([&](auto i_) { constexpr int j = decltype(i_)::v >> 1, hf = decltype(i_)::v & 1; const f16v& a = acc[2 * T + j];
          u4 u = {pk(a[8 * hf], a[8 * hf + 1]), pk(a[8 * hf + 2], a[8 * hf + 3]), pk(a[8 * hf + 4], a[8 * hf + 5]), pk(a[8 * hf + 6], a[8 * hf + 7])};
          __builtin_amdgcn_raw_buffer_store_b128(u, rsP, pv, ((4 * T + j) * 2 + hf) * 1024, SC1); }); };
      auto o_run = [&](auto t0_, auto n_, unsigned lb, bool last) { constexpr int T0 = decltype(t0_)::v, N = 8 * decltype(n_)::v;
        const unsigned x0 = opq(oa0 + lb), x1 = x0 ^ 64, y0 = opq(oa0 + YB), y1 = y0 ^ 64;
        bf8 a[N];
        auto rd = [&](auto i_) { constexpr int i = decltype(i_)::v;
          const unsigned ad = (i < 16 ? ((i & 4) ? x1 : x0) + (i >> 3) * US : ((i & 4) ? y1 : y0) + ((i >> 3) - 2) * US) + (i & 3) * 4096; a[i] = ldtr(sm, ad, ad + 1024); };
        sfor<0, PD>(rd); SB();
        sfor<0, N / 2>([&](auto j_) { constexpr int j = decltype(j_)::v, i = 2 * j, blk = 2 * T0 + (j >> 1), k = (j & 1) * 2;
          f16v c = mfma32(a[i + 1], ob[k + 1], acc[blk]); acc[blk] = mfma32(a[i], ob[k], c);
          if constexpr (i + PD < N) { rd(IC<i + PD>{}); rd(IC<i + PD + 1>{}); }
          SB();
          if constexpr ((j & 3) == 3) { if (last) store_t(IC<T0 + (j >> 2)>{}); }
          }); };
#pragma clang loop unroll(disable)
      for (int go = 0; go < 6; ++go) {
        go = opqs(go);
        const bool last = go == 5;
        if (!last) bar_v<8>();
        { const unsigned ad = opq(ha0 + (go & 1) * 8192); sfor<0, 4>([&](auto k_) { constexpr int k = decltype(k_)::v; ob[k] = ld128(sm, ad ^ (k << 5)); }); }
        o_run(IC<0>{}, IC<5>{}, last ? 0 : XB, last);
        if (last) break;
        seam(IC<0>{});
        while (!((chunks >> (k_p - 1)) & 1)) { bar_l(); seam(IC<0>{}); }
      }
    }
    bar_v<0>();
    bar_l();
    if (w == 0) reduce_slice(4);
  }
}

extern "C" void kernel_launch(void* const* d_in, const int* in_sizes, int n_in, void* d_out, int out_size, void* d_ws, size_t ws_size, hipStream_t stream) {
  fused_kernel<<<256, 512, 0, stream>>>((const char*)d_in[0], (const char*)d_in[1], (const char*)d_in[2], (const char*)d_in[3], (const char*)d_in[4], (char*)d_ws, (char*)d_out);
}
```
